```python
import torch

def _softmax(x, axis=-1):
    return torch.softmax(x, dim=axis)

def _gelu(x):
    return torch.nn.functional.gelu(x, approximate='tanh')

def _silu(x):
    return torch.nn.functional.silu(x)

def _sigmoid(x):
    return torch.sigmoid(x)

def _layernorm(x, axis=-1, eps=1e-05):
    m = x.mean(dim=axis, keepdim=True)
    v = x.var(dim=axis, keepdim=True, unbiased=False)
    return (x - m) / torch.sqrt(v + eps)

def _scatter_add(tgt, idx, src, axis=0):
    return tgt.clone().index_add_(axis, idx, src)

def _segment_sum(src, seg, n):
    out = torch.zeros((n,) + src.shape[1:], dtype=src.dtype)
    return out.index_add_(0, seg, src)

def _pad(x, pw):
    flat = tuple((v for pair in reversed(pw) for v in pair))
    return torch.nn.functional.pad(x, flat)
kernel_name = 'adaLN_cond__mha_block_cached__swiglu_ffn__decode'

def setup_inputs():

    _w = lambda a: (lambda t: t.to(torch.bfloat16) if t.is_floating_point() else t)(torch.as_tensor(a))

    def _A_setup():
        B, Sq, D = (1, 1, 640)
        H, Sk, Dh = (32, 32768, 192)
        return {'x': _w((lambda *s: torch.randn(*s, dtype=torch.bfloat16) * 0.1)(B, Sq, D)), 'k_cache': _w((lambda *s: torch.randn(*s, dtype=torch.bfloat16) * 0.8)(B, H, Sk, Dh)), 'v_cache': _w((lambda *s: torch.randn(*s, dtype=torch.bfloat16) * 0.8)(B, H, Sk, Dh)), 'wq': _w((lambda *s: torch.randn(*s, dtype=torch.bfloat16) * 0.8)(D, H * Dh) * D ** (-0.5)), 'wo': _w((lambda *s: torch.randn(*s, dtype=torch.bfloat16) * 0.8)(H * Dh, D) * (H * Dh) ** (-0.5))}
    _A_in = _A_setup()

    def _B_setup():
        B, S, D, F = (1, 1, 640, 1536)
        return {'x': _w((lambda *s: torch.randn(*s, dtype=torch.bfloat16) * 0.1)(B, S, D)), 'w_gate': _w((lambda *s: torch.randn(*s, dtype=torch.bfloat16) * 0.1)(D, F)), 'w_up': _w((lambda *s: torch.randn(*s, dtype=torch.bfloat16) * 0.1)(D, F)), 'w_down': _w((lambda *s: torch.randn(*s, dtype=torch.bfloat16) * 0.1)(F, D))}
    _B_in = _B_setup()
    return {'A_x': _A_in['x'], 'A_k_cache': _A_in['k_cache'], 'A_v_cache': _A_in['v_cache'], 'A_wq': _A_in['wq'], 'A_wo': _A_in['wo'], 'B_x': _B_in['x'], 'B_w_gate': _B_in['w_gate'], 'B_w_up': _B_in['w_up'], 'B_w_down': _B_in['w_down'], 'c': _w((lambda *s: torch.randn(*s, dtype=torch.bfloat16) * 0.8)(1, 640) * 8.0), 'w_mod': _w((lambda *s: torch.randn(*s, dtype=torch.bfloat16) * 0.8)(640, 6 * 640) * 640 ** (-0.5) * 8.0)}

def reference(A_x, A_k_cache, A_v_cache, A_wq, A_wo, B_x, B_w_gate, B_w_up, B_w_down, c, w_mod):

    def _A_ref(x, k_cache, v_cache, wq, wo):
        B, Sq, D = x.shape
        H, Dh = (32, 192)
        q = (lambda x, *d: x.permute(*d))((x @ wq).reshape(B, Sq, H, Dh), 0, 2, 1, 3)
        scores = torch.einsum('bhqd,bhkd->bhqk', q, k_cache) * Dh ** (-0.5)
        p = _softmax(scores, axis=-1)
        o = torch.einsum('bhqk,bhkd->bhqd', p, v_cache)
        return (lambda x, *d: x.permute(*d))(o, 0, 2, 1, 3).reshape(B, Sq, H * Dh) @ wo

    def _B_ref(x, w_gate, w_up, w_down):
        g = _silu(x @ w_gate)
        u = x @ w_up
        return g * u @ w_down
    x = A_x
    mod = (_silu(c) @ w_mod).reshape(c.shape[0], 6, 1, -1)
    sa, ca, ga, sb, cb, gb = (mod[:, 0].clone(), mod[:, 1].clone(), mod[:, 2].clone(), mod[:, 3].clone(), mod[:, 4].clone(), mod[:, 5].clone())
    h = _layernorm(x, axis=-1) * (1 + ca) + sa
    x = x + ga * _A_ref(h, A_k_cache, A_v_cache, A_wq, A_wo)
    h = _layernorm(x, axis=-1) * (1 + cb) + sb
    x = x + gb * _B_ref(h, B_w_gate, B_w_up, B_w_down)
    return x
```

```cpp
#include <hip/hip_runtime.h>
#include <hip/hip_bf16.h>
#include <cstdint>

#define MIMIC 0

typedef unsigned short u16;
using u32x4 = __attribute__((ext_vector_type(4))) unsigned;
using bf16x2 = __attribute__((ext_vector_type(2))) __bf16;

constexpr int D = 640, H = 32, SK = 32768, DH = 192, F = 1536, HD = H * DH;

__device__ __forceinline__ float bf2f(u16 v) { return __uint_as_float(((unsigned)v) << 16); }
__device__ __forceinline__ float rbf(float x) {
  return x;
}
__device__ __forceinline__ float lo16(unsigned v) { return __uint_as_float(v << 16); }
__device__ __forceinline__ float hi16(unsigned v) { return __uint_as_float(v & 0xffff0000u); }
__device__ __forceinline__ float dot2(unsigned a, unsigned b, float acc) {
  return __builtin_amdgcn_fdot2_f32_bf16(__builtin_bit_cast(bf16x2, a), __builtin_bit_cast(bf16x2, b), acc, false);
}
template <int CTRL> __device__ __forceinline__ float dpp(float x) {
  return __builtin_bit_cast(float, __builtin_amdgcn_mov_dpp(__builtin_bit_cast(int, x), CTRL, 0xf, 0xf, true));
}
__device__ __forceinline__ float silu_f(float x) { return x / (1.f + __expf(-x)); }

template <int K>
__device__ __forceinline__ float gemv64(const u16* __restrict__ W, int ldw, int col0, const float* in, float* red  ) {
  const int tid = threadIdx.x, c = tid & 7, r = tid >> 3;
  float acc[8];
#pragma unroll
  for (int i = 0; i < 8; ++i) acc[i] = 0.f;
#pragma unroll 4
  for (int k = r; k < K; k += 32) {
    u32x4 w = *reinterpret_cast<const u32x4*>(W + (size_t)k * ldw + col0 + c * 8);
    float s = in[k];
#pragma unroll
    for (int i = 0; i < 4; ++i) { acc[2 * i] = fmaf(s, lo16(w[i]), acc[2 * i]); acc[2 * i + 1] = fmaf(s, hi16(w[i]), acc[2 * i + 1]); }
  }
#pragma unroll
  for (int i = 0; i < 8; ++i) red[r * 64 + c * 8 + i] = acc[i];
  __syncthreads();
  float o = 0.f;
  if (tid < 64) {
#pragma unroll
    for (int g = 0; g < 32; ++g) o += red[g * 64 + tid];
  }
  __syncthreads();
  return o;
}

__device__ __forceinline__ float block_sum256(float v, float* tmp) {
  for (int o = 32; o > 0; o >>= 1) v += __shfl_xor(v, o);
  __syncthreads();
  if ((threadIdx.x & 63) == 0) tmp[threadIdx.x >> 6] = v;
  __syncthreads();
  return tmp[0] + tmp[1] + tmp[2] + tmp[3];
}

__device__ __forceinline__ void ln_mod(const float* xs, const float* ss, const float* cc, float* hs, float* tmp) {
  const int tid = threadIdx.x;
  float s = 0.f;
  for (int i = tid; i < D; i += 256) s += xs[i];
  const float mean = block_sum256(s, tmp) * (1.f / D);
  float v = 0.f;
  for (int i = tid; i < D; i += 256) { float d = xs[i] - mean; v += d * d; }
  const float var = block_sum256(v, tmp) * (1.f / D);
  const float mb = rbf(mean), vb = rbf(var);
  const float den = rbf(sqrtf(rbf(vb + 1e-5f)));
  for (int i = tid; i < D; i += 256) {
    float n = rbf(rbf(xs[i] - mb) / den);
    hs[i] = rbf(rbf(n * rbf(1.f + cc[i])) + ss[i]);
  }
  __syncthreads();
}

__global__ void __launch_bounds__(256) k_mod(const u16* c, const u16* wmod, float* mod) {
  __shared__ float in[D];
  __shared__ float red[32 * 64];
  for (int i = threadIdx.x; i < D; i += 256) in[i] = rbf(silu_f(bf2f(c[i])));
  __syncthreads();
  float o = gemv64<D>(wmod, 6 * D, blockIdx.x * 64, in, red);
  if (threadIdx.x < 64) mod[blockIdx.x * 64 + threadIdx.x] = rbf(o);
}

__global__ void __launch_bounds__(256) k_q(const u16* x, const float* mod, const u16* wq, u16* q) {
  __shared__ float xs[D], hs[D], tmp[4];
  __shared__ float red[32 * 64];
  for (int i = threadIdx.x; i < D; i += 256) xs[i] = bf2f(x[i]);
  __syncthreads();
  ln_mod(xs, mod, mod + D, hs, tmp);
  float o = gemv64<D>(wq, HD, blockIdx.x * 64, hs, red);
  if (threadIdx.x < 64) { __bf16 b = (__bf16)o; q[blockIdx.x * 64 + threadIdx.x] = __builtin_bit_cast(u16, b); }
}

constexpr int NSPLIT = 8, AW = 16;
constexpr int KEYS_PER_WG = SK / NSPLIT;
__global__ void __launch_bounds__(AW * 64) k_attn(const u16* __restrict__ q, const u16* __restrict__ K, const u16* __restrict__ V, float* __restrict__ part) {
  __shared__ float mrg[AW][200];
  const int h = blockIdx.x / NSPLIT, sp = blockIdx.x % NSPLIT;
  const int tid = threadIdx.x, lane = tid & 63, w = __builtin_amdgcn_readfirstlane(tid >> 6), r = lane >> 3, c = lane & 7;
  u32x4 qv[3];
#pragma unroll
  for (int j = 0; j < 3; ++j) qv[j] = *reinterpret_cast<const u32x4*>(q + h * DH + j * 64 + c * 8);
  const size_t base = ((size_t)h * SK + (size_t)sp * KEYS_PER_WG + w * 8 + r) * DH + c * 8;
  const u16* kp = K + base;
  const u16* vp = V + base;
  constexpr int NIT = KEYS_PER_WG / (AW * 8);
  constexpr size_t STEP = (size_t)AW * 8 * DH;
  float m = -INFINITY, l = 0.f, acc[24];
#pragma unroll
  for (int i = 0; i < 24; ++i) acc[i] = 0.f;
  const float scale = 0.07216878364870322f;
  u32x4 kA[3], vA[3], kB[3], vB[3];
#define LOAD(kk, vv, it) do { _Pragma("unroll") for (int j = 0; j < 3; ++j) kk[j] = __builtin_nontemporal_load(reinterpret_cast<const u32x4*>(kp + (size_t)(it) * STEP + j * 64)); \
    _Pragma("unroll") for (int j = 0; j < 3; ++j) vv[j] = __builtin_nontemporal_load(reinterpret_cast<const u32x4*>(vp + (size_t)(it) * STEP + j * 64)); } while (0)
#define COMP(kk, vv) do { float d = 0.f; \
    _Pragma("unroll") for (int j = 0; j < 3; ++j) _Pragma("unroll") for (int i = 0; i < 4; ++i) d = dot2(kk[j][i], qv[j][i], d); \
    d += dpp<0xB1>(d); d += dpp<0x4E>(d); d += dpp<0x141>(d); \
    const float s = rbf(rbf(d) * scale); \
    const float mn = fmaxf(m, s); const float al = __expf(m - mn); const float p = __expf(s - mn); m = mn; l = fmaf(l, al, p); \
    _Pragma("unroll") for (int j = 0; j < 3; ++j) _Pragma("unroll") for (int i = 0; i < 4; ++i) { \
      acc[j * 8 + 2 * i] = fmaf(p, lo16(vv[j][i]), acc[j * 8 + 2 * i] * al); acc[j * 8 + 2 * i + 1] = fmaf(p, hi16(vv[j][i]), acc[j * 8 + 2 * i + 1] * al); } } while (0)
  LOAD(kA, vA, 0);
  for (int it = 0; it < NIT; it += 2) {
    LOAD(kB, vB, it + 1);
    COMP(kA, vA);
    if (it + 2 < NIT) LOAD(kA, vA, it + 2);
    COMP(kB, vB);
  }
  float M = m;
  M = fmaxf(M, __shfl_xor(M, 8)); M = fmaxf(M, __shfl_xor(M, 16)); M = fmaxf(M, __shfl_xor(M, 32));
  const float sc = __expf(m - M);
  l *= sc;
  l += __shfl_xor(l, 8); l += __shfl_xor(l, 16); l += __shfl_xor(l, 32);
#pragma unroll
  for (int i = 0; i < 24; ++i) { float a = acc[i] * sc; a += __shfl_xor(a, 8); a += __shfl_xor(a, 16); a += __shfl_xor(a, 32); acc[i] = a; }
  if (r == 0) {
#pragma unroll
    for (int j = 0; j < 3; ++j)
#pragma unroll
      for (int i = 0; i < 8; ++i) mrg[w][8 + j * 64 + c * 8 + i] = acc[j * 8 + i];
    if (c == 0) { mrg[w][0] = M; mrg[w][1] = l; }
  }
  __syncthreads();
  if (tid < DH) {
    float MM = -INFINITY;
#pragma unroll
    for (int i = 0; i < AW; ++i) MM = fmaxf(MM, mrg[i][0]);
    float L = 0.f, o = 0.f;
#pragma unroll
    for (int i = 0; i < AW; ++i) { float e = __expf(mrg[i][0] - MM); L = fmaf(e, mrg[i][1], L); o = fmaf(e, mrg[i][8 + tid], o); }
    float* pp = part + (size_t)blockIdx.x * 200;
    pp[8 + tid] = o;
    if (tid == 0) { pp[0] = MM; pp[1] = L; }
  }
}

__global__ void __launch_bounds__(256) k_wo(const float* part, const u16* wo, float* ypart) {
  __shared__ float os[DH];
  __shared__ float red[32 * 64];
  const int h = blockIdx.x, cg = blockIdx.y, tid = threadIdx.x;
  if (tid < DH) {
    const float* pp = part + (size_t)h * NSPLIT * 200;
    float MM = -INFINITY;
    for (int s = 0; s < NSPLIT; ++s) MM = fmaxf(MM, pp[s * 200]);
    float L = 0.f, o = 0.f;
    for (int s = 0; s < NSPLIT; ++s) { float e = __expf(pp[s * 200] - MM); L = fmaf(e, pp[s * 200 + 1], L); o = fmaf(e, pp[s * 200 + 8 + tid], o); }
    os[tid] = rbf(o / L);
  }
  __syncthreads();
  float o = gemv64<DH>(wo + (size_t)h * DH * D, D, cg * 64, os, red);
  if (tid < 64) ypart[h * D + cg * 64 + tid] = o;
}

__global__ void __launch_bounds__(256) k_gu(const u16* x, const float* mod, const float* ypart, const u16* wg, const u16* wu, float* x1out, float* a) {
  __shared__ float xs[D], hs[D], tmp[4];
  __shared__ float red[32 * 64];
  for (int i = threadIdx.x; i < D; i += 256) {
    float y = 0.f;
    for (int h = 0; h < H; ++h) y += ypart[h * D + i];
    float x1 = rbf(bf2f(x[i]) + rbf(mod[2 * D + i] * rbf(y)));
    xs[i] = x1;
    if (blockIdx.x == 0) x1out[i] = x1;
  }
  __syncthreads();
  ln_mod(xs, mod + 3 * D, mod + 4 * D, hs, tmp);
  float g = gemv64<D>(wg, F, blockIdx.x * 64, hs, red);
  float u = gemv64<D>(wu, F, blockIdx.x * 64, hs, red);
  if (threadIdx.x < 64) a[blockIdx.x * 64 + threadIdx.x] = rbf(rbf(silu_f(rbf(g))) * rbf(u));
}

__global__ void __launch_bounds__(256) k_down(const float* a, const float* x1, const float* mod, const u16* wd, u16* out) {
  __shared__ float in[F];
  __shared__ float red[32 * 64];
  for (int i = threadIdx.x; i < F; i += 256) in[i] = a[i];
  __syncthreads();
  float f = gemv64<F>(wd, D, blockIdx.x * 64, in, red);
  if (threadIdx.x < 64) {
    int n = blockIdx.x * 64 + threadIdx.x;
    float o = x1[n] + rbf(mod[5 * D + n] * rbf(f));
    __bf16 b = (__bf16)o; out[n] = __builtin_bit_cast(u16, b);
  }
}

extern "C" void kernel_launch(void* const* d_in, const int* in_sizes, int n_in, void* d_out, int out_size, void* d_ws, size_t ws_size, hipStream_t stream) {
  const u16* x = (const u16*)d_in[0];
  const u16* K = (const u16*)d_in[1];
  const u16* V = (const u16*)d_in[2];
  const u16* wq = (const u16*)d_in[3];
  const u16* wo = (const u16*)d_in[4];
  const u16* wg = (const u16*)d_in[6];
  const u16* wu = (const u16*)d_in[7];
  const u16* wd = (const u16*)d_in[8];
  const u16* c = (const u16*)d_in[9];
  const u16* wmod = (const u16*)d_in[10];
  char* ws = (char*)d_ws;
  float* mod = (float*)ws;
  u16* q = (u16*)(ws + 16384);
  float* part = (float*)(ws + 32768);
  float* ypart = (float*)(ws + 262144);
  float* x1 = (float*)(ws + 393216);
  float* a = (float*)(ws + 397312);
  k_mod<<<60, 256, 0, stream>>>(c, wmod, mod);
  k_q<<<96, 256, 0, stream>>>(x, mod, wq, q);
  k_attn<<<H * NSPLIT, AW * 64, 0, stream>>>(q, K, V, part);
  k_wo<<<dim3(H, 10), 256, 0, stream>>>(part, wo, ypart);
  k_gu<<<F / 64, 256, 0, stream>>>(x, mod, ypart, wg, wu, x1, a);
  k_down<<<D / 64, 256, 0, stream>>>(a, x1, mod, wd, (u16*)d_out);
}
```

```cpp
#include <hip/hip_runtime.h>
#include <cstdint>

typedef unsigned short u16;
typedef unsigned long long u64;
using u32x4 = __attribute__((ext_vector_type(4))) unsigned;
using u32x2 = __attribute__((ext_vector_type(2))) unsigned;
using bf16x2 = __attribute__((ext_vector_type(2))) __bf16;

#define KU 2
#define SPCT 75
#define STAMPS 0

constexpr int D = 640, H = 32, SK = 32768, DH = 192, F = 1536, HD = H * DH;
constexpr int NWG = 256, NRED = 160, NFFN = 48, FFN0 = 160, M20 = 208, NM2 = 40, NP1 = 64;
constexpr unsigned WS_EPOCH = 0;
constexpr unsigned WS_CTR = 1024;
constexpr unsigned WS_P1 = 16384;
constexpr unsigned WS_P2 = WS_P1 + 8 * 8 * 160 * 16;
constexpr unsigned WS_M2 = WS_P2 + 32 * 8 * 192 * 16;
constexpr unsigned WS_R = WS_M2 + 2560 * 8;
constexpr unsigned WS_Y = WS_R + 256 * 200 * 16;
constexpr unsigned WS_X1 = WS_Y + 160 * 32 * 64;
constexpr unsigned WS_O = WS_X1 + 640 * 8;
constexpr unsigned WS_ST = WS_O + 160 * 48 * 64;
constexpr unsigned WS_END = WS_ST + 256 * 16 * 8;

__device__ __forceinline__ float rbf(float x) { return (float)(__bf16)x; }
__device__ __forceinline__ unsigned bfbits(float x) { return (unsigned)__builtin_bit_cast(u16, (__bf16)x); }
__device__ __forceinline__ float lo16(unsigned v) { return __uint_as_float(v << 16); }
__device__ __forceinline__ float hi16(unsigned v) { return __uint_as_float(v & 0xffff0000u); }
__device__ __forceinline__ float dot2(unsigned a, unsigned b, float acc) {
  return __builtin_amdgcn_fdot2_f32_bf16(__builtin_bit_cast(bf16x2, a), __builtin_bit_cast(bf16x2, b), acc, false);
}
template <int CTRL> __device__ __forceinline__ float dpp(float x) {
  return __builtin_bit_cast(float, __builtin_amdgcn_mov_dpp(__builtin_bit_cast(int, x), CTRL, 0xf, 0xf, true));
}
constexpr int XOR1 = 0xB1, XOR2 = 0x4E, HMIR = 0x141, ROR8 = 0x128, ROR4 = 0x124;
__device__ __forceinline__ float tr32(float a, float b) {
  auto s = __builtin_amdgcn_permlane32_swap(__float_as_uint(a), __float_as_uint(b), false, false);
  return __uint_as_float(s[0]) + __uint_as_float(s[1]);
}
__device__ __forceinline__ float tr16(float a, float b) {
  auto s = __builtin_amdgcn_permlane16_swap(__float_as_uint(a), __float_as_uint(b), false, false);
  return __uint_as_float(s[0]) + __uint_as_float(s[1]);
}
__device__ __forceinline__ float tr8(float a, float b, bool bit3) { const float t1 = a + dpp<ROR8>(a), t2 = b + dpp<ROR8>(b); return bit3 ? t2 : t1; }
__device__ __forceinline__ float mx32(float a) { auto s = __builtin_amdgcn_permlane32_swap(__float_as_uint(a), __float_as_uint(a), false, false); return fmaxf(__uint_as_float(s[0]), __uint_as_float(s[1])); }
__device__ __forceinline__ float mx16(float a) { auto s = __builtin_amdgcn_permlane16_swap(__float_as_uint(a), __float_as_uint(a), false, false); return fmaxf(__uint_as_float(s[0]), __uint_as_float(s[1])); }
__device__ __forceinline__ float sum_hi3(float a) { a += dpp<ROR8>(a); a = tr16(a, a); return tr32(a, a); }
__device__ __forceinline__ float max_hi3(float a) { a = fmaxf(a, dpp<ROR8>(a)); return mx32(mx16(a)); }
__device__ __forceinline__ float sum_row16(float a) { a += dpp<XOR1>(a); a += dpp<XOR2>(a); a += dpp<HMIR>(a); a += dpp<ROR8>(a); return a; }
__device__ __forceinline__ float sum_wave(float a) { a = sum_row16(a); a = tr16(a, a); return tr32(a, a); }
__device__ __forceinline__ unsigned plo(unsigned r1, unsigned r0) { return __builtin_amdgcn_perm(r1, r0, 0x05040100u); }
__device__ __forceinline__ unsigned phi(unsigned r1, unsigned r0) { return __builtin_amdgcn_perm(r1, r0, 0x07060302u); }
__device__ __forceinline__ float silu_f(float x) { return x / (1.f + __expf(-x)); }
__device__ __forceinline__ void bar() { asm volatile("s_waitcnt lgkmcnt(0)" ::: "memory"); __builtin_amdgcn_s_barrier(); asm volatile("" ::: "memory"); }

typedef __amdgpu_buffer_rsrc_t rsrc_t;
__device__ __forceinline__ rsrc_t mk(const void* p, unsigned bytes) { return __builtin_amdgcn_make_buffer_rsrc((void*)p, 0, bytes, 0x00020000); }
constexpr int AUX_NT = 2, AUX_SC1 = 16;

struct Tag { unsigned tl, th; };
__device__ __forceinline__ u32x4 enc2(float v, Tag t) { const unsigned b = __float_as_uint(v); return u32x4{(b & 0xffffu) | t.tl, t.th, (b >> 16) | t.tl, t.th}; }
__device__ __forceinline__ float dec2(u32x4 g) { return __uint_as_float((g[0] & 0xffffu) | (g[2] << 16)); }
__device__ __forceinline__ unsigned decp(u32x4 g) { return (g[0] & 0xffffu) | (g[2] << 16); }
__device__ __forceinline__ void st16(rsrc_t r, unsigned off, u32x4 v) { __builtin_amdgcn_raw_buffer_store_b128(v, r, off, 0, AUX_SC1); }
__device__ __forceinline__ void st8(rsrc_t r, unsigned off, u32x2 v) { __builtin_amdgcn_raw_buffer_store_b64(v, r, off, 0, AUX_SC1); }

constexpr u64 SPIN_LIMIT = 20000000ull;
template <int N>
__device__ __forceinline__ void gather(rsrc_t r, unsigned off, unsigned stride, Tag t, u32x4 (&g)[N], u64 t_start) {
  while (true) {
#pragma unroll
    for (int k = 0; k < N; ++k) g[k] = __builtin_amdgcn_raw_buffer_load_b128(r, off + k * stride, 0, AUX_SC1);
    unsigned bad = 0;
#pragma unroll
    for (int k = 0; k < N; ++k) bad |= (((g[k][0] ^ t.tl) | (g[k][2] ^ t.tl)) >> 16) | (g[k][1] ^ t.th) | (g[k][3] ^ t.th);
    if (__builtin_amdgcn_ballot_w64(bad != 0u) == 0ull) break;
    asm volatile("s_sleep 1" ::: "memory");
    if (wall_clock64() - t_start > SPIN_LIMIT) break;
  }
}
constexpr unsigned OOB = 0xfffffff0u;

#define STAMP(i) do { } while (0)

constexpr int L_RED = 0;
constexpr int L_HS = L_RED + 1920;
constexpr int L_Q = L_HS + 40;
constexpr int L_PART = L_Q + 96;
constexpr int L_OP = L_PART + 800;
constexpr int L_H2 = L_OP + 96;
constexpr int L_GU = L_H2 + 1280;
constexpr int L_M2 = L_GU + 256;
constexpr int L_CNT = L_M2 + 256;
constexpr int L_WD = L_CNT + 4;
constexpr int L_TOTAL = 24576;
static_assert(L_WD % 4 == 0 && L_WD + 10240 <= L_TOTAL, "lds map");

__global__ void __launch_bounds__(512, 2)
k_block(const u16* __restrict__ x, const u16* __restrict__ Kc, const u16* __restrict__ Vc, const u16* __restrict__ wq, const u16* __restrict__ wo,
        const u16* __restrict__ wg, const u16* __restrict__ wu, const u16* __restrict__ wd, const u16* __restrict__ cnd, const u16* __restrict__ wmod,
        u16* __restrict__ out, unsigned char* __restrict__ ws) {
  __shared__ __attribute__((aligned(16))) unsigned lds[L_TOTAL];
  float* ldf = reinterpret_cast<float*>(lds);
  const int tid = threadIdx.x, lane = tid & 63, wv = __builtin_amdgcn_readfirstlane(tid >> 6);
  const int b = blockIdx.x, h = b >> 3, j = b & 7;

  if (wv < 4) {
    constexpr int U = KU;
    constexpr int NW = 32;
    constexpr int TPG = SK / (8 * U);
    constexpr int S = (TPG / NW) * SPCT / 100;
    const int r = lane >> 3, c = lane & 7, rnk = j * 4 + wv;
    const unsigned loff = r * 384 + c * 16;
    const rsrc_t rk = mk((const char*)Kc + (size_t)h * SK * 384, SK * 384), rv = mk((const char*)Vc + (size_t)h * SK * 384, SK * 384);
    const unsigned* cp = (const unsigned*)(ws + WS_CTR + h * 256);
    u32x4 A[2 * U][3], B[2 * U][3];
#define LOADK(X, tile) do { const unsigned so_ = (unsigned)(tile) * (3072u * U); \
    _Pragma("unroll") for (int u = 0; u < U; ++u) _Pragma("unroll") for (int q_ = 0; q_ < 3; ++q_) X[u][q_] = __builtin_amdgcn_raw_buffer_load_b128(rk, loff + u * 3072 + q_ * 128, so_, AUX_NT); } while (0)
#define LOADV(X, tile) do { const unsigned so_ = (unsigned)(tile) * (3072u * U); \
    _Pragma("unroll") for (int u = 0; u < U; ++u) _Pragma("unroll") for (int q_ = 0; q_ < 3; ++q_) X[U + u][q_] = __builtin_amdgcn_raw_buffer_load_b128(rv, loff + u * 3072 + q_ * 128, so_, AUX_NT); } while (0)
#define LOADT(X, tile) do { LOADK(X, tile); LOADV(X, tile); } while (0)
#define USE(X) do { float s_[U]; float mn_ = m; \
    _Pragma("unroll") for (int u = 0; u < U; ++u) { float d_ = 0.f; \
      _Pragma("unroll") for (int q_ = 0; q_ < 3; ++q_) _Pragma("unroll") for (int i = 0; i < 4; ++i) d_ = dot2(X[u][q_][i], qv[q_][i], d_); \
      d_ += dpp<XOR1>(d_); d_ += dpp<XOR2>(d_); d_ += dpp<HMIR>(d_); \
      s_[u] = rbf(rbf(d_) * 0.07216878364870322f); mn_ = fmaxf(mn_, s_[u]); } \
    const float al_ = __expf(m - mn_); m = mn_; l *= al_; \
    _Pragma("unroll") for (int i = 0; i < 24; ++i) acc[i] *= al_; \
    _Pragma("unroll") for (int u = 0; u < U; ++u) { const float p_ = __expf(s_[u] - mn_); l += p_; \
      _Pragma("unroll") for (int q_ = 0; q_ < 3; ++q_) _Pragma("unroll") for (int i = 0; i < 4; ++i) { \
        acc[q_ * 8 + 2 * i] = fmaf(p_, lo16(X[U + u][q_][i]), acc[q_ * 8 + 2 * i]); acc[q_ * 8 + 2 * i + 1] = fmaf(p_, hi16(X[U + u][q_][i]), acc[q_ * 8 + 2 * i + 1]); } } } while (0)
#define GRAB(gg) do { u64 sv_; asm volatile("s_mov_b64 %1, exec\n\ts_mov_b64 exec, 1\n\tglobal_atomic_add %0, %2, %3, %4 sc0\n\ts_mov_b64 exec, %1" : "=&v"(gg), "=&s"(sv_) : "v"(0u), "v"(1u), "s"(cp) : "memory"); } while (0)
    int tA = rnk, tB = NW + rnk, n = 2;
    bar();
    LOADT(A, tA);
    bar();
    bar();
    LOADT(B, tB);
    bar();
    bar();
    u32x4 qv[3];
#pragma unroll
    for (int q_ = 0; q_ < 3; ++q_) qv[q_] = *reinterpret_cast<const u32x4*>(lds + L_Q + q_ * 32 + c * 4);
    if (wv == 0) STAMP(1);
    float m = -INFINITY, l = 0.f, acc[24];
#pragma unroll
    for (int i = 0; i < 24; ++i) acc[i] = 0.f;
    USE(A);
    tA = 2 * NW + rnk; n = 3;
    while (true) {
      unsigned g2 = 0;
      const bool dyn2 = n >= S;
      if (dyn2) GRAB(g2);
      LOADT(A, tA);
      USE(B);
      asm volatile("s_waitcnt vmcnt(%1)" : "+v"(g2) : "i"(6 * U) : "memory");
      tB = dyn2 ? S * NW + (int)__builtin_amdgcn_readfirstlane(g2) : n * NW + rnk; ++n;
      if (tB >= TPG) { USE(A); break; }
      unsigned g1 = 0;
      const bool dyn1 = n >= S;
      if (dyn1) GRAB(g1);
      LOADT(B, tB);
      USE(A);
      asm volatile("s_waitcnt vmcnt(%1)" : "+v"(g1) : "i"(6 * U) : "memory");
      tA = dyn1 ? S * NW + (int)__builtin_amdgcn_readfirstlane(g1) : n * NW + rnk; ++n;
      if (tA >= TPG) { USE(B); break; }
    }
    const float M = max_hi3(m);
    const float sc = __expf(m - M);
    const float L = sum_hi3(l * sc);
    float y1[12], y2[6], y3[3];
#pragma unroll
    for (int k = 0; k < 12; ++k) y1[k] = tr32(acc[k] * sc, acc[k + 12] * sc);
#pragma unroll
    for (int k = 0; k < 6; ++k) y2[k] = tr16(y1[k], y1[k + 6]);
#pragma unroll
    for (int k = 0; k < 3; ++k) y3[k] = tr8(y2[k], y2[k + 3], (lane & 8) != 0);
    const int ai0 = 12 * (lane >> 5) + 6 * ((lane >> 4) & 1) + 3 * ((lane >> 3) & 1);
#pragma unroll
    for (int k = 0; k < 3; ++k) { const int ai = ai0 + k; ldf[L_PART + wv * 200 + (ai >> 3) * 64 + c * 8 + (ai & 7)] = y3[k]; }
    if (lane == 0) { ldf[L_PART + wv * 200 + 192] = M; ldf[L_PART + wv * 200 + 193] = L; }
    if (wv == 0) STAMP(2);
    bar();
    return;
  }

  const int vt = tid - 256, vw = wv - 4;
  const u64 t_start = wall_clock64();
  if (vw == 0) STAMP(0);
  const rsrc_t rws = mk(ws, WS_END);
  const u32x2 ev = __builtin_amdgcn_raw_buffer_load_b64(rws, WS_EPOCH, 0, AUX_SC1);
  const rsrc_t rmod = mk(wmod, D * 6 * D * 2), rwq = mk(wq, D * HD * 2);
  u32x4 wm[8]; unsigned cpair[4];
  const bool p1 = b < NP1 && vt < 200;
  {
    const int cc = vt % 20, rg = vt / 20, kb = b >> 3;
    const unsigned col = cc < 10 ? 80 * j + 8 * cc : 640 + 80 * j + 8 * (cc - 10);
    if (p1) {
#pragma unroll
      for (int k = 0; k < 4; ++k) cpair[k] = *reinterpret_cast<const unsigned*>(cnd + 80 * kb + 2 * (rg + 10 * k));
#pragma unroll
      for (int k = 0; k < 8; ++k) wm[k] = __builtin_amdgcn_raw_buffer_load_b128(rmod, ((80 * kb + 2 * (rg + 10 * (k >> 1)) + (k & 1)) * (6 * D) + col) * 2, 0, AUX_NT);
    }
  }
  unsigned xp[5];
#pragma unroll
  for (int k = 0; k < 5; ++k) xp[k] = *reinterpret_cast<const unsigned*>(x + 2 * (lane + 64 * k));
  const float xi = __uint_as_float((unsigned)x[80 * j + (vt < 160 ? vt >> 1 : 0)] << 16);
  u32x4 wqr[8];
  const int qc = vt % 24, qr = vt / 24;
  if (vt < 240) {
#pragma unroll
    for (int k = 0; k < 8; ++k) wqr[k] = __builtin_amdgcn_raw_buffer_load_b128(rwq, ((80 * j + 2 * (qr + 10 * (k >> 1)) + (k & 1)) * HD + h * DH + 8 * qc) * 2, 0, AUX_NT);
  }
  if (j == 0 && vt == 0) (void)__hip_atomic_exchange((unsigned*)(ws + WS_CTR + h * 256), 0u, __ATOMIC_RELAXED, __HIP_MEMORY_SCOPE_AGENT);
  if (vt == 255) lds[L_CNT] = 0u;
  bar();
  const u64 E1 = (((u64)ev[1] << 32) | ev[0]) + 1ull;
  Tag tg; tg.tl = (unsigned)(E1 & 0xffffu) << 16; tg.th = (unsigned)(E1 >> 16);
  if (p1) {
    float a8[8];
#pragma unroll
    for (int i = 0; i < 8; ++i) a8[i] = 0.f;
#pragma unroll
    for (int k = 0; k < 4; ++k) {
      const unsigned sp = bfbits(silu_f(lo16(cpair[k]))) | (bfbits(silu_f(hi16(cpair[k]))) << 16);
#pragma unroll
      for (int i = 0; i < 4; ++i) { a8[2 * i] = dot2(plo(wm[2 * k + 1][i], wm[2 * k][i]), sp, a8[2 * i]); a8[2 * i + 1] = dot2(phi(wm[2 * k + 1][i], wm[2 * k][i]), sp, a8[2 * i + 1]); }
    }
    const int cc = vt % 20, rg = vt / 20;
#pragma unroll
    for (int i = 0; i < 8; ++i) ldf[L_RED + rg * 192 + cc * 8 + i] = a8[i];
  }
  bar();
  if (b < NP1 && vt < 160) {
    float s = 0.f;
#pragma unroll
    for (int g = 0; g < 10; ++g) s += ldf[L_RED + g * 192 + vt];
    st16(rws, WS_P1 + ((j * 8 + (b >> 3)) * 160 + vt) * 16, enc2(s, tg));
  }
  float mb, den;
  {
    float s = 0.f;
#pragma unroll
    for (int k = 0; k < 5; ++k) s += lo16(xp[k]) + hi16(xp[k]);
    const float mean = sum_wave(s) * (1.f / D);
    float v = 0.f;
#pragma unroll
    for (int k = 0; k < 5; ++k) { const float d0 = lo16(xp[k]) - mean, d1 = hi16(xp[k]) - mean; v = fmaf(d0, d0, v); v = fmaf(d1, d1, v); }
    const float var = sum_wave(v) * (1.f / D);
    mb = rbf(mean); den = rbf(sqrtf(rbf(rbf(var) + 1e-5f)));
  }
  if (vw < 3) {
    u32x4 g[8];
    const int vc = vt < 160 ? vt : 159;
    gather<8>(rws, WS_P1 + (j * 8 * 160 + (vc >> 1) + 80 * (vc & 1)) * 16, 160 * 16, tg, g, t_start);
    float sa = 0.f;
#pragma unroll
    for (int k = 0; k < 8; ++k) sa += dec2(g[k]);
    const float ca = dpp<XOR1>(sa);
    const float nrm = rbf(rbf(xi - mb) / den);
    const float hv = rbf(rbf(nrm * rbf(1.f + rbf(ca))) + rbf(sa));
    if (vt < 160 && (vt & 1) == 0) reinterpret_cast<u16*>(lds + L_HS)[vt >> 1] = (u16)bfbits(hv);
  }
  asm volatile("s_waitcnt vmcnt(0)" ::: "memory");
  bar();
  if (vw == 0) STAMP(3);
  if (vt < 240) {
    float a8[8];
#pragma unroll
    for (int i = 0; i < 8; ++i) a8[i] = 0.f;
#pragma unroll
    for (int k = 0; k < 4; ++k) {
      const unsigned hp = lds[L_HS + qr + 10 * k];
#pragma unroll
      for (int i = 0; i < 4; ++i) { a8[2 * i] = dot2(plo(wqr[2 * k + 1][i], wqr[2 * k][i]), hp, a8[2 * i]); a8[2 * i + 1] = dot2(phi(wqr[2 * k + 1][i], wqr[2 * k][i]), hp, a8[2 * i + 1]); }
    }
#pragma unroll
    for (int i = 0; i < 8; ++i) ldf[L_RED + qr * 192 + qc * 8 + i] = a8[i];
  }
  bar();
  if (vw < 3) {
    float s = 0.f;
#pragma unroll
    for (int g = 0; g < 10; ++g) s += ldf[L_RED + g * 192 + vt];
    st16(rws, WS_P2 + (b * 192 + vt) * 16, enc2(s, tg));
    u32x4 g[8];
    gather<8>(rws, WS_P2 + (h * 8 * 192 + vt) * 16, 192 * 16, tg, g, t_start);
    float qs = 0.f;
#pragma unroll
    for (int k = 0; k < 8; ++k) qs += dec2(g[k]);
    reinterpret_cast<u16*>(lds + L_Q)[vt] = (u16)bfbits(qs);
  }
  bar();

  if (b >= M20 && b < M20 + NM2) {
    const int rg = lane >> 3, cc = lane & 7, col = 2 * D + (b - M20) * 64 + cc * 8;
    u32x4 w[20]; unsigned cp2[10];
#pragma unroll
    for (int k = 0; k < 10; ++k) cp2[k] = *reinterpret_cast<const unsigned*>(cnd + 160 * vw + 2 * (rg + 8 * k));
#pragma unroll
    for (int k = 0; k < 20; ++k) w[k] = __builtin_amdgcn_raw_buffer_load_b128(rmod, ((160 * vw + 2 * (rg + 8 * (k >> 1)) + (k & 1)) * (6 * D) + col) * 2, 0, AUX_NT);
    float a8[8];
#pragma unroll
    for (int i = 0; i < 8; ++i) a8[i] = 0.f;
#pragma unroll
    for (int k = 0; k < 10; ++k) {
      const unsigned sp = bfbits(silu_f(lo16(cp2[k]))) | (bfbits(silu_f(hi16(cp2[k]))) << 16);
#pragma unroll
      for (int i = 0; i < 4; ++i) { a8[2 * i] = dot2(plo(w[2 * k + 1][i], w[2 * k][i]), sp, a8[2 * i]); a8[2 * i + 1] = dot2(phi(w[2 * k + 1][i], w[2 * k][i]), sp, a8[2 * i + 1]); }
    }
    float z1[4], z2[2];
#pragma unroll
    for (int k = 0; k < 4; ++k) z1[k] = tr32(a8[k], a8[k + 4]);
#pragma unroll
    for (int k = 0; k < 2; ++k) z2[k] = tr16(z1[k], z1[k + 2]);
    const float z3 = tr8(z2[0], z2[1], (lane & 8) != 0);
    ldf[L_M2 + vw * 64 + cc * 8 + 4 * (lane >> 5) + 2 * ((lane >> 4) & 1) + ((lane >> 3) & 1)] = z3;
    asm volatile("s_waitcnt lgkmcnt(0)" ::: "memory");
    unsigned old = 0;
    if (lane == 0) old = __hip_atomic_fetch_add(lds + L_CNT, 1u, __ATOMIC_RELAXED, __HIP_MEMORY_SCOPE_WORKGROUP);
    old = __builtin_amdgcn_readfirstlane(old);
    asm volatile("" ::: "memory");
    if (old == 3u) {
      const float s = (ldf[L_M2 + lane] + ldf[L_M2 + 64 + lane]) + (ldf[L_M2 + 128 + lane] + ldf[L_M2 + 192 + lane]);
      st8(rws, WS_M2 + ((b - M20) * 64 + lane) * 8, u32x2{bfbits(s) | tg.tl, tg.th});
    }
  }
  const int org = lane / 10, ocg = lane - 10 * org;
  unsigned wop[4][8];
  {
    const rsrc_t rwo = mk(wo, HD * D * 2);
    const unsigned o0 = lane < 60 ? ((h * DH + 48 * vw + 8 * org) * D + 80 * j + 8 * ocg) * 2 : OOB;
    u32x4 w[8];
#pragma unroll
    for (int k = 0; k < 8; ++k) w[k] = __builtin_amdgcn_raw_buffer_load_b128(rwo, o0, k * D * 2, AUX_NT);
#pragma unroll
    for (int p = 0; p < 4; ++p)
#pragma unroll
      for (int i = 0; i < 4; ++i) { wop[p][2 * i] = plo(w[2 * p + 1][i], w[2 * p][i]); wop[p][2 * i + 1] = phi(w[2 * p + 1][i], w[2 * p][i]); }
  }
  auto h1_stage = [&]() {
    if (vw == 0) STAMP(4);
    {
      const float m0 = ldf[L_PART + 192], m1 = ldf[L_PART + 200 + 192], m2 = ldf[L_PART + 400 + 192], m3 = ldf[L_PART + 600 + 192];
      const float M = fmaxf(fmaxf(m0, m1), fmaxf(m2, m3));
      const float e0 = __expf(m0 - M), e1 = __expf(m1 - M), e2 = __expf(m2 - M), e3 = __expf(m3 - M);
      const int idx = vt < 192 ? vt : 193;
      float v = e0 * ldf[L_PART + idx];
      v = fmaf(e1, ldf[L_PART + 200 + idx], v); v = fmaf(e2, ldf[L_PART + 400 + idx], v); v = fmaf(e3, ldf[L_PART + 600 + idx], v);
      if (vt == 192) v = M;
      if (vt < 194) st16(rws, WS_R + (b * 200 + vt) * 16, enc2(v, tg));
    }
    {
      const int s = lane >> 3, dg = lane & 7;
      u32x4 g[8];
      const unsigned rb = WS_R + (h * 8 + s) * 200 * 16;
      while (true) {
#pragma unroll
        for (int k = 0; k < 6; ++k) g[k] = __builtin_amdgcn_raw_buffer_load_b128(rws, rb + (48 * vw + 6 * dg + k) * 16, 0, AUX_SC1);
#pragma unroll
        for (int k = 0; k < 2; ++k) g[6 + k] = __builtin_amdgcn_raw_buffer_load_b128(rws, rb + (192 + k) * 16, 0, AUX_SC1);
        unsigned bad = 0;
#pragma unroll
        for (int k = 0; k < 8; ++k) bad |= (((g[k][0] ^ tg.tl) | (g[k][2] ^ tg.tl)) >> 16) | (g[k][1] ^ tg.th) | (g[k][3] ^ tg.th);
        if (__builtin_amdgcn_ballot_w64(bad != 0u) == 0ull) break;
        asm volatile("s_sleep 1" ::: "memory");
        if (wall_clock64() - t_start > SPIN_LIMIT) break;
      }
      const float ms = dec2(g[6]), M = max_hi3(ms), e = __expf(ms - M);
      const float L = sum_hi3(e * dec2(g[7]));
      float z[3];
#pragma unroll
      for (int k = 0; k < 3; ++k) z[k] = tr32(e * dec2(g[k]), e * dec2(g[k + 3]));
      const float z01 = tr16(z[0], z[1]), z2 = tr16(z[2], z[2]);
      const float o01 = z01 + dpp<ROR8>(z01), o2 = z2 + dpp<ROR8>(z2);
      u16* op = reinterpret_cast<u16*>(lds + L_OP + vw * 24) + 6 * dg + 3 * (lane >> 5);
      if ((lane & 8) == 0) op[(lane >> 4) & 1] = (u16)bfbits(o01 / L);
      else if ((lane & 16) == 0) op[2] = (u16)bfbits(o2 / L);
      asm volatile("s_waitcnt lgkmcnt(0)" ::: "memory");
      const u32x4 opv = *reinterpret_cast<const u32x4*>(lds + L_OP + vw * 24 + 4 * (org < 6 ? org : 0));
      float y[8];
#pragma unroll
      for (int i = 0; i < 8; ++i) y[i] = 0.f;
#pragma unroll
      for (int p = 0; p < 4; ++p)
#pragma unroll
        for (int i = 0; i < 8; ++i) y[i] = dot2(wop[p][i], opv[p], y[i]);
      if (lane < 60) {
        float* dst = ldf + L_RED + (vw * 6 + org) * 80 + 8 * ocg;
        *reinterpret_cast<float4*>(dst) = float4{y[0], y[1], y[2], y[3]};
        *reinterpret_cast<float4*>(dst + 4) = float4{y[4], y[5], y[6], y[7]};
      }
    }
    bar();
    if (vt < 80) {
      float p4[4] = {0.f, 0.f, 0.f, 0.f};
#pragma unroll
      for (int g = 0; g < 24; ++g) p4[g & 3] += ldf[L_RED + g * 80 + vt];
      st16(rws, WS_Y + (((20 * j + (vt >> 2)) * 32 + h) * 4 + (vt & 3)) * 16, enc2((p4[0] + p4[1]) + (p4[2] + p4[3]), tg));
    }
    if (vw == 0) STAMP(5);
  };

  if (b < NRED) {
    const int col = 4 * b + (lane & 3);
    float ga = 0.f, gb = 0.f, xv = 0.f;
    if (vw == 3) {
      u32x4 g[2];
      gather<2>(rws, WS_M2 + (col & ~1) * 8, 3 * D * 8, tg, g, t_start);
      const unsigned pa = decp(g[0]), pb = decp(g[1]);
      ga = (col & 1) ? hi16(pa) : lo16(pa); gb = (col & 1) ? hi16(pb) : lo16(pb);
      xv = __uint_as_float((unsigned)x[col] << 16);
    }
    bar();
    h1_stage();
    if (vw != 3) return;
    float x1;
    {
      u32x4 g[2];
      gather<2>(rws, WS_Y + (b * 128 + lane) * 16, 64 * 16, tg, g, t_start);
      float y = dec2(g[0]) + dec2(g[1]);
      y += dpp<ROR4>(y); y = sum_hi3(y);
      x1 = rbf(xv + rbf(ga * rbf(y)));
      if (lane < 4) st8(rws, WS_X1 + col * 8, u32x2{bfbits(x1) | tg.tl, tg.th});
    }
    STAMP(6);
    {
      u32x4 g[3];
      gather<3>(rws, WS_O + (b * NFFN * 4 + lane) * 16, 64 * 16, tg, g, t_start);
      float f = (dec2(g[0]) + dec2(g[1])) + dec2(g[2]);
      f += dpp<ROR4>(f); f = sum_hi3(f);
      const float o = x1 + rbf(gb * rbf(f));
      if (lane < 4) out[col] = (u16)bfbits(o);
      if (b == 0 && lane == 0) *reinterpret_cast<u64*>(ws + WS_EPOCH) = E1;
    }
    STAMP(7);
  } else if (b < FFN0 + NFFN) {
    const int f = b - FFN0, rg = vt >> 3, cc = vt & 7;
    if (vw < 3) {
      const rsrc_t rwd = mk(wd, F * D * 2);
      const unsigned o0 = vt < 160 ? (32 * f * D + 4 * vt) * 2 : OOB;
      u32x2 w[32];
#pragma unroll
      for (int k = 0; k < 32; ++k) w[k] = __builtin_amdgcn_raw_buffer_load_b64(rwd, o0, k * D * 2, AUX_NT);
      if (vt < 160) {
#pragma unroll
        for (int p = 0; p < 16; ++p)
          *reinterpret_cast<u32x4*>(lds + L_WD + (p * 160 + vt) * 4) =
              u32x4{plo(w[2 * p + 1][0], w[2 * p][0]), phi(w[2 * p + 1][0], w[2 * p][0]), plo(w[2 * p + 1][1], w[2 * p][1]), phi(w[2 * p + 1][1], w[2 * p][1])};
      }
    }
    unsigned gp[10][8];
    {
      const u16* base = (cc < 4 ? wg : wu) + 32 * f + 8 * (cc & 3);
      u32x4 w[20];
#pragma unroll
      for (int k = 0; k < 20; ++k) w[k] = __builtin_nontemporal_load(reinterpret_cast<const u32x4*>(base + (size_t)(20 * rg + k) * F));
#pragma unroll
      for (int p = 0; p < 10; ++p)
#pragma unroll
        for (int i = 0; i < 4; ++i) { gp[p][2 * i] = plo(w[2 * p + 1][i], w[2 * p][i]); gp[p][2 * i + 1] = phi(w[2 * p + 1][i], w[2 * p][i]); }
    }
    unsigned sbp[5], cbp[5];
    {
      u32x4 g[10];
      gather<10>(rws, WS_M2 + (D + 2 * lane) * 8, 128 * 8, tg, g, t_start);
#pragma unroll
      for (int k = 0; k < 5; ++k) { sbp[k] = decp(g[k]); cbp[k] = decp(g[5 + k]); }
    }
    bar();
    h1_stage();
    u32x4 wdr[16];
#pragma unroll
    for (int p = 0; p < 8; ++p) wdr[p] = *reinterpret_cast<const u32x4*>(lds + L_WD + (p * 160 + (vt < 160 ? vt : 0)) * 4);
    {
      u32x4 g[5];
      gather<5>(rws, WS_X1 + 2 * lane * 8, 128 * 8, tg, g, t_start);
      if (vw == 0) STAMP(6);
      unsigned x1p[5];
#pragma unroll
      for (int k = 0; k < 5; ++k) x1p[k] = decp(g[k]);
      float s = 0.f;
#pragma unroll
      for (int k = 0; k < 5; ++k) s += lo16(x1p[k]) + hi16(x1p[k]);
      const float mean = sum_wave(s) * (1.f / D);
      float v = 0.f;
#pragma unroll
      for (int k = 0; k < 5; ++k) { const float d0 = lo16(x1p[k]) - mean, d1 = hi16(x1p[k]) - mean; v = fmaf(d0, d0, v); v = fmaf(d1, d1, v); }
      const float var = sum_wave(v) * (1.f / D);
      const float mb2 = rbf(mean), den2 = rbf(sqrtf(rbf(rbf(var) + 1e-5f)));
#pragma unroll
      for (int k = 0; k < 5; ++k) {
        const float n0 = rbf(rbf(lo16(x1p[k]) - mb2) / den2), n1 = rbf(rbf(hi16(x1p[k]) - mb2) / den2);
        const float h0 = rbf(n0 * rbf(1.f + lo16(cbp[k]))) + lo16(sbp[k]), h1 = rbf(n1 * rbf(1.f + hi16(cbp[k]))) + hi16(sbp[k]);
        lds[L_H2 + vw * 320 + lane + 64 * k] = bfbits(h0) | (bfbits(h1) << 16);
      }
    }
    asm volatile("s_waitcnt lgkmcnt(0)" ::: "memory");
    {
      float a8[8];
#pragma unroll
      for (int i = 0; i < 8; ++i) a8[i] = 0.f;
#pragma unroll
      for (int p2 = 0; p2 < 5; ++p2) {
        const u32x2 hp = *reinterpret_cast<const u32x2*>(lds + L_H2 + vw * 320 + 10 * rg + 2 * p2);
#pragma unroll
        for (int i = 0; i < 8; ++i) { a8[i] = dot2(gp[2 * p2][i], hp[0], a8[i]); a8[i] = dot2(gp[2 * p2 + 1][i], hp[1], a8[i]); }
      }
      asm volatile("" ::: "memory");
#pragma unroll
      for (int p = 8; p < 16; ++p) wdr[p] = *reinterpret_cast<const u32x4*>(lds + L_WD + (p * 160 + (vt < 160 ? vt : 0)) * 4);
      float z1[4], z2[2];
#pragma unroll
      for (int k = 0; k < 4; ++k) z1[k] = tr32(a8[k], a8[k + 4]);
#pragma unroll
      for (int k = 0; k < 2; ++k) z2[k] = tr16(z1[k], z1[k + 2]);
      const float z3 = tr8(z2[0], z2[1], (lane & 8) != 0);
      ldf[L_GU + vw * 64 + cc * 8 + 4 * (lane >> 5) + 2 * ((lane >> 4) & 1) + ((lane >> 3) & 1)] = z3;
    }
    bar();
    if (vw < 3) {
      const int n = lane & 31;
      const float g = (ldf[L_GU + n] + ldf[L_GU + 64 + n]) + (ldf[L_GU + 128 + n] + ldf[L_GU + 192 + n]);
      const float u = (ldf[L_GU + 32 + n] + ldf[L_GU + 96 + n]) + (ldf[L_GU + 160 + n] + ldf[L_GU + 224 + n]);
      const unsigned ab = bfbits(rbf(silu_f(rbf(g))) * rbf(u));
      const unsigned nb = (unsigned)__builtin_amdgcn_mov_dpp((int)ab, XOR1, 0xf, 0xf, true);
      const unsigned ap = ab | (nb << 16);
      float y[4] = {0.f, 0.f, 0.f, 0.f};
#pragma unroll
      for (int p = 0; p < 16; ++p) {
        const unsigned a2 = (unsigned)__builtin_amdgcn_readlane((int)ap, 2 * p);
#pragma unroll
        for (int i = 0; i < 4; ++i) y[i] = dot2(wdr[p][i], a2, y[i]);
      }
      if (vt < 160) {
#pragma unroll
        for (int i = 0; i < 4; ++i) st16(rws, WS_O + ((vt * NFFN + f) * 4 + i) * 16, enc2(y[i], tg));
      }
    }
    if (vw == 0) STAMP(7);
  } else {
    bar();
    h1_stage();
  }
}

extern "C" void kernel_launch(void* const* d_in, const int* in_sizes, int n_in, void* d_out, int out_size, void* d_ws, size_t ws_size, hipStream_t stream) {
  (void)in_sizes; (void)n_in; (void)out_size; (void)ws_size;
  k_block<<<NWG, 512, 0, stream>>>((const u16*)d_in[0], (const u16*)d_in[1], (const u16*)d_in[2], (const u16*)d_in[3], (const u16*)d_in[4],
                                   (const u16*)d_in[6], (const u16*)d_in[7], (const u16*)d_in[8], (const u16*)d_in[9], (const u16*)d_in[10],
                                   (u16*)d_out, (unsigned char*)d_ws);
}
```

```cpp
#include <hip/hip_runtime.h>
#include <cstdint>

typedef unsigned short u16;
typedef unsigned long long u64;
using u32x4 = __attribute__((ext_vector_type(4))) unsigned;
using u32x2 = __attribute__((ext_vector_type(2))) unsigned;
using bf16x2 = __attribute__((ext_vector_type(2))) __bf16;

#define KU 2
#define SPCT 75
#define PF 6
#define NL 2
#define PSLEEP 10
#define ROT 0
#define STAMPS 0

constexpr int D = 640, H = 32, SK = 32768, DH = 192, F = 1536, HD = H * DH;
constexpr int NWG = 256, NFFN = 64;
constexpr unsigned WS_EPOCH = 0;
constexpr unsigned WS_CTR = 1024;
constexpr unsigned WS_P1 = 16384;
constexpr unsigned WS_P2 = WS_P1 + 8 * 8 * 160 * 16;
constexpr unsigned WS_M2 = WS_P2 + 32 * 8 * 192 * 16;
constexpr unsigned WS_R = WS_M2 + 2560 * 8;
constexpr unsigned WS_Y = WS_R + 256 * 200 * 16;
constexpr unsigned WS_X1 = WS_Y + 160 * 32 * 64;
constexpr unsigned WS_O = WS_X1 + 640 * 8;
constexpr unsigned WS_ST = WS_O + 160 * NFFN * 64;
constexpr unsigned WS_END = WS_ST + 256 * 16 * 8;

__device__ __forceinline__ float rbf(float x) { return (float)(__bf16)x; }
__device__ __forceinline__ unsigned bfbits(float x) { return (unsigned)__builtin_bit_cast(u16, (__bf16)x); }
__device__ __forceinline__ float lo16(unsigned v) { return __uint_as_float(v << 16); }
__device__ __forceinline__ float hi16(unsigned v) { return __uint_as_float(v & 0xffff0000u); }
__device__ __forceinline__ float dot2(unsigned a, unsigned b, float acc) {
  return __builtin_amdgcn_fdot2_f32_bf16(__builtin_bit_cast(bf16x2, a), __builtin_bit_cast(bf16x2, b), acc, false);
}
template <int CTRL> __device__ __forceinline__ float dpp(float x) {
  return __builtin_bit_cast(float, __builtin_amdgcn_mov_dpp(__builtin_bit_cast(int, x), CTRL, 0xf, 0xf, true));
}
constexpr int XOR1 = 0xB1, XOR2 = 0x4E, HMIR = 0x141, ROR8 = 0x128, ROR4 = 0x124;
__device__ __forceinline__ float tr32(float a, float b) {
  auto s = __builtin_amdgcn_permlane32_swap(__float_as_uint(a), __float_as_uint(b), false, false);
  return __uint_as_float(s[0]) + __uint_as_float(s[1]);
}
__device__ __forceinline__ float tr16(float a, float b) {
  auto s = __builtin_amdgcn_permlane16_swap(__float_as_uint(a), __float_as_uint(b), false, false);
  return __uint_as_float(s[0]) + __uint_as_float(s[1]);
}
__device__ __forceinline__ float tr8(float a, float b, bool bit3) { const float t1 = a + dpp<ROR8>(a), t2 = b + dpp<ROR8>(b); return bit3 ? t2 : t1; }
__device__ __forceinline__ float mx32(float a) { auto s = __builtin_amdgcn_permlane32_swap(__float_as_uint(a), __float_as_uint(a), false, false); return fmaxf(__uint_as_float(s[0]), __uint_as_float(s[1])); }
__device__ __forceinline__ float mx16(float a) { auto s = __builtin_amdgcn_permlane16_swap(__float_as_uint(a), __float_as_uint(a), false, false); return fmaxf(__uint_as_float(s[0]), __uint_as_float(s[1])); }
__device__ __forceinline__ float sum_hi3(float a) { a += dpp<ROR8>(a); a = tr16(a, a); return tr32(a, a); }
__device__ __forceinline__ float max_hi3(float a) { a = fmaxf(a, dpp<ROR8>(a)); return mx32(mx16(a)); }
__device__ __forceinline__ float sum_row16(float a) { a += dpp<XOR1>(a); a += dpp<XOR2>(a); a += dpp<HMIR>(a); a += dpp<ROR8>(a); return a; }
__device__ __forceinline__ float sum_wave(float a) { a = sum_row16(a); a = tr16(a, a); return tr32(a, a); }
__device__ __forceinline__ unsigned plo(unsigned r1, unsigned r0) { return __builtin_amdgcn_perm(r1, r0, 0x05040100u); }
__device__ __forceinline__ unsigned phi(unsigned r1, unsigned r0) { return __builtin_amdgcn_perm(r1, r0, 0x07060302u); }
__device__ __forceinline__ float silu_f(float x) { return x / (1.f + __expf(-x)); }
__device__ __forceinline__ void bar() { asm volatile("s_waitcnt lgkmcnt(0)" ::: "memory"); __builtin_amdgcn_s_barrier(); asm volatile("" ::: "memory"); }

typedef __amdgpu_buffer_rsrc_t rsrc_t;
__device__ __forceinline__ rsrc_t mk(const void* p, unsigned bytes) { return __builtin_amdgcn_make_buffer_rsrc((void*)p, 0, bytes, 0x00020000); }
constexpr int AUX_NT = 2, AUX_SC1 = 16;

struct Tag { unsigned tl, th; };
__device__ __forceinline__ u32x4 enc2(float v, Tag t) { const unsigned b = __float_as_uint(v); return u32x4{(b & 0xffffu) | t.tl, t.th, (b >> 16) | t.tl, t.th}; }
__device__ __forceinline__ float dec2(u32x4 g) { return __uint_as_float((g[0] & 0xffffu) | (g[2] << 16)); }
__device__ __forceinline__ unsigned decp(u32x4 g) { return (g[0] & 0xffffu) | (g[2] << 16); }
__device__ __forceinline__ void st16(rsrc_t r, unsigned off, u32x4 v) { __builtin_amdgcn_raw_buffer_store_b128(v, r, off, 0, AUX_SC1); }
__device__ __forceinline__ void st8(rsrc_t r, unsigned off, u32x2 v) { __builtin_amdgcn_raw_buffer_store_b64(v, r, off, 0, AUX_SC1); }

constexpr unsigned SPIN_LIMIT = 1u << 19;
constexpr unsigned OOB = 0x80000000u;
__device__ __forceinline__ unsigned bad2(u32x4 g, Tag t) { return (((g[0] ^ t.tl) | (g[2] ^ t.tl)) >> 16) | (g[1] ^ t.th) | (g[3] ^ t.th); }
template <int N, class LD>
__device__ __forceinline__ void poll(LD ld, Tag t, u32x4 (&g)[N]) {
  u32x4 y[N];
#pragma unroll
  for (int k = 0; k < N; ++k) g[k] = ld(k);
  asm volatile("s_sleep %0" :: "i"(PSLEEP) : "memory");
  for (unsigned spin = 0; spin < SPIN_LIMIT; ++spin) {
#pragma unroll
    for (int k = 0; k < N; ++k) y[k] = ld(k);
    unsigned bad = 0;
#pragma unroll
    for (int k = 0; k < N; ++k) bad |= bad2(g[k], t);
    if (__builtin_amdgcn_ballot_w64(bad != 0u) == 0ull) break;
    asm volatile("" ::: "memory");
#pragma unroll
    for (int k = 0; k < N; ++k) g[k] = ld(k);
    bad = 0;
#pragma unroll
    for (int k = 0; k < N; ++k) bad |= bad2(y[k], t);
    if (__builtin_amdgcn_ballot_w64(bad != 0u) == 0ull) {
#pragma unroll
      for (int k = 0; k < N; ++k) g[k] = y[k];
      break;
    }
    asm volatile("" ::: "memory");
  }
}
template <int N>
__device__ __forceinline__ void gather(rsrc_t r, unsigned off, unsigned stride, Tag t, u32x4 (&g)[N]) {
  poll<N>([&](int k) { return __builtin_amdgcn_raw_buffer_load_b128(r, off + k * stride, 0, AUX_SC1); }, t, g);
}
template <int N>
__device__ __forceinline__ void gather_slow(rsrc_t r, const unsigned (&off)[N], Tag t, u32x4 (&g)[N]) {
  for (unsigned spin = 0; spin < SPIN_LIMIT; ++spin) {
#pragma unroll
    for (int k = 0; k < N; ++k) g[k] = __builtin_amdgcn_raw_buffer_load_b128(r, off[k], 0, AUX_SC1);
    unsigned bad = 0;
#pragma unroll
    for (int k = 0; k < N; ++k) bad |= bad2(g[k], t);
    if (__builtin_amdgcn_ballot_w64(bad != 0u) == 0ull) break;
    asm volatile("s_sleep 16" ::: "memory");
  }
}
__device__ __forceinline__ void lds_wait() { asm volatile("s_waitcnt lgkmcnt(0)" ::: "memory"); }
__device__ __forceinline__ float fdiv(float a, float d, float r) { const float q = a * r; return fmaf(fmaf(-q, d, a), r, q); }

#define STAMP(i) do { } while (0)

constexpr int L_RED = 0;
constexpr int L_HS = L_RED + 1920;
constexpr int L_Q = L_HS + 40;
constexpr int L_PART = L_Q + 96;
constexpr int L_OP = L_PART + 800;
constexpr int L_H2 = L_OP + 96;
constexpr int L_GU = L_H2 + 320;
constexpr int L_CNT = L_GU + 256;
constexpr int L_WD = L_CNT + 4;
constexpr int L_KV = L_WD + 7680;
constexpr int KVW = (NL > 0 ? NL : 1) * KU * 1536;
constexpr int L_TOTAL = L_KV + 4 * KVW < 24576 ? 24576 : L_KV + 4 * KVW;
static_assert(L_WD % 4 == 0 && L_KV % 4 == 0 && L_TOTAL * 4 <= 163840, "lds map");

__global__ void __launch_bounds__(512, 2)
k_block(const u16* __restrict__ x, const u16* __restrict__ Kc, const u16* __restrict__ Vc, const u16* __restrict__ wq, const u16* __restrict__ wo,
        const u16* __restrict__ wg, const u16* __restrict__ wu, const u16* __restrict__ wd, const u16* __restrict__ cnd, const u16* __restrict__ wmod,
        u16* __restrict__ out, unsigned char* __restrict__ ws) {
  __shared__ __attribute__((aligned(16))) unsigned lds[L_TOTAL];
  float* ldf = reinterpret_cast<float*>(lds);
  const int tid = threadIdx.x, lane = tid & 63, wv = __builtin_amdgcn_readfirstlane(tid >> 6);
  const int b = blockIdx.x, h = b >> 3, j = b & 7;
  const rsrc_t rws = mk(ws, WS_END);
  const rsrc_t rmod = mk(wmod, D * 6 * D * 2), rc = mk(cnd, D * 2);
  const int mrg = lane / 5, mcc = lane - 5 * mrg;
  const int gl = lane < 40 ? lane : 39;

  if (wv < 4) {
    constexpr int U = KU;
    constexpr int NW = 32;
    constexpr int TPG = SK / (8 * U);
    constexpr int S = (TPG / NW) * SPCT / 100;
    static_assert(S >= NL + 4, "queue");
    const int r = lane >> 3, c = lane & 7, rnk = j * 4 + wv;
    const unsigned loff = r * 384 + c * 16;
    const rsrc_t rk = mk((const char*)Kc + (size_t)h * SK * 384, SK * 384), rv = mk((const char*)Vc + (size_t)h * SK * 384, SK * 384);
    const unsigned* cp = (const unsigned*)(ws + WS_CTR + h * 256);
    if (j < 2) {
      const int j1 = (2 * h + j) & 7, kb = h >> 2;
      const unsigned col = (wv >> 1) * D + 80 * j1 + 40 * (wv & 1) + 8 * mcc;
      const unsigned ob = lane < 50 ? 0u : OOB;
      u32x4 wm[8]; unsigned cpair[4];
#pragma unroll
      for (int k = 0; k < 8; ++k) wm[k] = __builtin_amdgcn_raw_buffer_load_b128(rmod, ((80 * kb + 2 * (mrg + 10 * (k >> 1)) + (k & 1)) * (6 * D) + col) * 2 | ob, 0, AUX_NT);
      asm volatile("" ::: "memory");
#pragma unroll
      for (int k = 0; k < 4; ++k) cpair[k] = __builtin_amdgcn_raw_buffer_load_b32(rc, (80 * kb + 2 * (mrg + 10 * k)) * 2 | ob, 0, 0);
      const u32x2 ev = __builtin_amdgcn_raw_buffer_load_b64(rws, WS_EPOCH, 0, AUX_SC1);
      bar();
      float a8[8];
#pragma unroll
      for (int i = 0; i < 8; ++i) a8[i] = 0.f;
#pragma unroll
      for (int k = 0; k < 4; ++k) {
        const unsigned sp = bfbits(silu_f(lo16(cpair[k]))) | (bfbits(silu_f(hi16(cpair[k]))) << 16);
#pragma unroll
        for (int i = 0; i < 4; ++i) { a8[2 * i] = dot2(plo(wm[2 * k + 1][i], wm[2 * k][i]), sp, a8[2 * i]); a8[2 * i + 1] = dot2(phi(wm[2 * k + 1][i], wm[2 * k][i]), sp, a8[2 * i + 1]); }
      }
      float* wred = ldf + L_KV + wv * KVW;
      if (lane < 50) {
        *reinterpret_cast<float4*>(wred + mrg * 40 + mcc * 8) = float4{a8[0], a8[1], a8[2], a8[3]};
        *reinterpret_cast<float4*>(wred + mrg * 40 + mcc * 8 + 4) = float4{a8[4], a8[5], a8[6], a8[7]};
      }
      lds_wait();
      float s = 0.f;
#pragma unroll
      for (int g = 0; g < 10; ++g) s += wred[g * 40 + gl];
      const u64 E1 = (((u64)ev[1] << 32) | ev[0]) + 1ull;
      Tag tg; tg.tl = (unsigned)(E1 & 0xffffu) << 16; tg.th = (unsigned)(E1 >> 16);
      if (lane < 40) st16(rws, WS_P1 + ((j1 * 8 + kb) * 160 + 40 * wv + lane) * 16, enc2(s, tg));
      if (wv == 0) STAMP(11);
      asm volatile("s_waitcnt lgkmcnt(0)" ::: "memory");
    } else {
      bar();
    }
    u32x4 A[2 * U][3], B[2 * U][3];
#define TOFF(tile) ((unsigned)(ROT ? ((tile) + h * ROT) & (TPG - 1) : (tile)) * (3072u * U))
#define LOADT(X, tile) do { const unsigned so_ = TOFF(tile), vo_ = (tile) < TPG ? loff : OOB; \
    _Pragma("unroll") for (int u = 0; u < 2 * U; ++u) _Pragma("unroll") for (int q_ = 0; q_ < 3; ++q_) \
      X[u][q_] = __builtin_amdgcn_raw_buffer_load_b128(u < U ? rk : rv, vo_ + (u % U) * 3072 + q_ * 128, so_, AUX_NT); } while (0)
#define LOADP(X, tile, i0) do { const unsigned so_ = TOFF(tile); \
    _Pragma("unroll") for (int u = 0; u < 2 * U; ++u) _Pragma("unroll") for (int q_ = 0; q_ < 3; ++q_) { \
      if ((i0) + u * 3 + q_ >= PF) asm volatile("s_waitcnt vmcnt(%0)" :: "i"(PF - 1) : "memory"); \
      X[u][q_] = __builtin_amdgcn_raw_buffer_load_b128(u < U ? rk : rv, loff + (u % U) * 3072 + q_ * 128, so_, AUX_NT); } } while (0)
#define USE(X) do { float s_[U]; float mn_ = m; \
    _Pragma("unroll") for (int u = 0; u < U; ++u) { float d_ = 0.f; \
      _Pragma("unroll") for (int q_ = 0; q_ < 3; ++q_) _Pragma("unroll") for (int i = 0; i < 4; ++i) d_ = dot2(X[u][q_][i], qv[q_][i], d_); \
      d_ += dpp<XOR1>(d_); d_ += dpp<XOR2>(d_); d_ += dpp<HMIR>(d_); \
      s_[u] = rbf(rbf(d_) * 0.07216878364870322f); mn_ = fmaxf(mn_, s_[u]); } \
    const float al_ = __expf(m - mn_); m = mn_; l *= al_; \
    _Pragma("unroll") for (int i = 0; i < 24; ++i) acc[i] *= al_; \
    _Pragma("unroll") for (int u = 0; u < U; ++u) { const float p_ = __expf(s_[u] - mn_); l += p_; \
      _Pragma("unroll") for (int q_ = 0; q_ < 3; ++q_) _Pragma("unroll") for (int i = 0; i < 4; ++i) { \
        acc[q_ * 8 + 2 * i] = fmaf(p_, lo16(X[U + u][q_][i]), acc[q_ * 8 + 2 * i]); acc[q_ * 8 + 2 * i + 1] = fmaf(p_, hi16(X[U + u][q_][i]), acc[q_ * 8 + 2 * i + 1]); } } } while (0)
#define GRAB(gg) do { u64 sv_; asm volatile("s_mov_b64 %1, exec\n\ts_mov_b64 exec, 1\n\tglobal_atomic_add %0, %2, %3, %4 sc0\n\ts_mov_b64 exec, %1" : "=&v"(gg), "=&s"(sv_) : "v"(0u), "v"(1u), "s"(cp) : "memory"); } while (0)
    LOADP(B, rnk, 0);
    LOADP(A, NW + rnk, 6 * U);
    const unsigned lbase = (unsigned)(L_KV + wv * KVW) * 4u;
#pragma unroll
    for (int t = 0; t < NL; ++t) {
      const unsigned so_ = TOFF((2 + t) * NW + rnk);
#pragma unroll
      for (int u = 0; u < 2 * U; ++u)
#pragma unroll
        for (int q_ = 0; q_ < 3; ++q_) {
          unsigned keep;
          asm volatile("s_waitcnt vmcnt(%6)\n\ts_mov_b32 %0, m0\n\ts_mov_b32 m0, %1\n\ts_nop 4\n\tbuffer_load_dwordx4 %2, %3, %4 offen nt lds\n\ts_mov_b32 m0, %0"
                       : "=&s"(keep) : "s"(lbase + (unsigned)((t * 2 * U + u) * 3 + q_) * 1024u), "v"(loff), "s"(u < U ? rk : rv),
                         "s"(so_ + (unsigned)((u % U) * 3072 + q_ * 128)), "i"(0), "i"(PF - 1) : "memory");
        }
    }
    if (wv == 0) STAMP(8);
    bar();
    u32x4 qv[3];
#pragma unroll
    for (int q_ = 0; q_ < 3; ++q_) qv[q_] = *reinterpret_cast<const u32x4*>(lds + L_Q + q_ * 32 + c * 4);
    if (wv == 0) STAMP(1);
    float m = -INFINITY, l = 0.f, acc[24];
#pragma unroll
    for (int i = 0; i < 24; ++i) acc[i] = 0.f;
    int tA = 0, tB, n = 2 + NL;
    unsigned gA = 0, gB = 0;
    do {
      USE(B);
      asm volatile("s_waitcnt vmcnt(%1)" : "+v"(gB) : "i"(6 * U) : "memory");
      tB = tA >= TPG ? TPG : n >= S ? S * NW + (int)__builtin_amdgcn_readfirstlane(gB) : n * NW + rnk; ++n;
      if (n >= S && tB < TPG) GRAB(gA);
      LOADT(B, tB);
      asm volatile("" ::: "memory");
      if (tA < TPG) USE(A);
      asm volatile("s_waitcnt vmcnt(%1)" : "+v"(gA) : "i"(6 * U) : "memory");
      tA = tB >= TPG ? TPG : n >= S ? S * NW + (int)__builtin_amdgcn_readfirstlane(gA) : n * NW + rnk; ++n;
      if (n >= S && tA < TPG) GRAB(gB);
      LOADT(A, tA);
      asm volatile("" ::: "memory");
      if (NL > 0 && n == 4 + NL) {
        asm volatile("s_waitcnt vmcnt(%0)" :: "i"(12 * U) : "memory");
#pragma unroll 1
        for (int t = 0; t < NL; ++t) {
          u32x4 C[2 * U][3];
#pragma unroll
          for (int u = 0; u < 2 * U; ++u)
#pragma unroll
            for (int q_ = 0; q_ < 3; ++q_) C[u][q_] = *reinterpret_cast<const u32x4*>(lds + L_KV + wv * KVW + ((t * 2 * U + u) * 3 + q_) * 256 + lane * 4);
          USE(C);
        }
      }
    } while (tB < TPG);
    const float M = max_hi3(m);
    const float sc = __expf(m - M);
    const float L = sum_hi3(l * sc);
    float y1[12], y2[6], y3[3];
#pragma unroll
    for (int k = 0; k < 12; ++k) y1[k] = tr32(acc[k] * sc, acc[k + 12] * sc);
#pragma unroll
    for (int k = 0; k < 6; ++k) y2[k] = tr16(y1[k], y1[k + 6]);
#pragma unroll
    for (int k = 0; k < 3; ++k) y3[k] = tr8(y2[k], y2[k + 3], (lane & 8) != 0);
    const int ai0 = 12 * (lane >> 5) + 6 * ((lane >> 4) & 1) + 3 * ((lane >> 3) & 1);
#pragma unroll
    for (int k = 0; k < 3; ++k) { const int ai = ai0 + k; ldf[L_PART + wv * 200 + (ai >> 3) * 64 + c * 8 + (ai & 7)] = y3[k]; }
    if (lane == 0) { ldf[L_PART + wv * 200 + 192] = M; ldf[L_PART + wv * 200 + 193] = L; }
    if (wv == 0) STAMP(2);
    bar();
    return;
  }

  const int vt = tid - 256, vw = wv - 4;
  if (vw == 0) STAMP(0);
  float* wred = ldf + L_RED + vw * 480;
  const u32x2 ev = __builtin_amdgcn_raw_buffer_load_b64(rws, WS_EPOCH, 0, AUX_SC1);
  unsigned xp[5];
#pragma unroll
  for (int k = 0; k < 5; ++k) xp[k] = *reinterpret_cast<const unsigned*>(x + 2 * (lane + 64 * k));
  const float xi = __uint_as_float((unsigned)x[80 * j + 20 * vw + (gl >> 1)] << 16);
  const int qr = lane / 6, qc = lane - 6 * qr;
  unsigned wqp[4][8];
  {
    const rsrc_t rwq = mk(wq, D * HD * 2);
    const unsigned ob = lane < 60 ? 0u : OOB;
    u32x4 wqr[8];
#pragma unroll
    for (int k = 0; k < 8; ++k) wqr[k] = __builtin_amdgcn_raw_buffer_load_b128(rwq, ((80 * j + 2 * (qr + 10 * (k >> 1)) + (k & 1)) * HD + h * DH + 48 * vw + 8 * qc) * 2 | ob, 0, AUX_NT);
    if (j == 0 && vt == 0) (void)__hip_atomic_exchange((unsigned*)(ws + WS_CTR + h * 256), 0u, __ATOMIC_RELAXED, __HIP_MEMORY_SCOPE_AGENT);
    if (vt == 255) { lds[L_CNT] = 0u; lds[L_CNT + 1] = 0u; }
    bar();
    if (vw == 0) STAMP(9);
#pragma unroll
    for (int k = 0; k < 4; ++k)
#pragma unroll
      for (int i = 0; i < 4; ++i) { wqp[k][2 * i] = plo(wqr[2 * k + 1][i], wqr[2 * k][i]); wqp[k][2 * i + 1] = phi(wqr[2 * k + 1][i], wqr[2 * k][i]); }
#pragma unroll
    for (int k = 0; k < 4; ++k)
#pragma unroll
      for (int i = 0; i < 8; ++i) asm volatile("" : "+v"(wqp[k][i]));
  }
  const u64 E1 = (((u64)ev[1] << 32) | ev[0]) + 1ull;
  Tag tg; tg.tl = (unsigned)(E1 & 0xffffu) << 16; tg.th = (unsigned)(E1 >> 16);
  float nrm;
  {
    float s = 0.f;
#pragma unroll
    for (int k = 0; k < 5; ++k) s += lo16(xp[k]) + hi16(xp[k]);
    const float mean = sum_wave(s) * (1.f / D);
    float v = 0.f;
#pragma unroll
    for (int k = 0; k < 5; ++k) { const float d0 = lo16(xp[k]) - mean, d1 = hi16(xp[k]) - mean; v = fmaf(d0, d0, v); v = fmaf(d1, d1, v); }
    const float var = sum_wave(v) * (1.f / D);
    const float mb = rbf(mean), den = rbf(sqrtf(rbf(rbf(var) + 1e-5f)));
    nrm = rbf(rbf(xi - mb) / den);
    asm volatile("s_waitcnt vmcnt(0)" : "+v"(nrm) :: "memory");
  }
  {
    u32x4 g[8];
    gather<8>(rws, WS_P1 + (j * 8 * 160 + 20 * vw + (gl >> 1) + 80 * (gl & 1)) * 16, 160 * 16, tg, g);
    const float sa = ((dec2(g[0]) + dec2(g[1])) + (dec2(g[2]) + dec2(g[3]))) + ((dec2(g[4]) + dec2(g[5])) + (dec2(g[6]) + dec2(g[7])));
    const float ca = dpp<XOR1>(sa);
    const float hv = rbf(nrm * rbf(1.f + rbf(ca))) + rbf(sa);
    if (lane < 40 && (lane & 1) == 0) reinterpret_cast<u16*>(lds + L_HS)[20 * vw + (lane >> 1)] = (u16)bfbits(hv);
    lds_wait();
    if (lane == 0) (void)__hip_atomic_fetch_add(lds + L_CNT, 1u, __ATOMIC_RELAXED, __HIP_MEMORY_SCOPE_WORKGROUP);
    if (vw == 0) STAMP(12);
    for (unsigned spin = 0; spin < (SPIN_LIMIT << 4); ++spin)
      if (__hip_atomic_load(lds + L_CNT, __ATOMIC_RELAXED, __HIP_MEMORY_SCOPE_WORKGROUP) >= 4u) break;
    asm volatile("" ::: "memory");
  }
  {
    float a8[8];
#pragma unroll
    for (int i = 0; i < 8; ++i) a8[i] = 0.f;
#pragma unroll
    for (int k = 0; k < 4; ++k) {
      const unsigned hp = lds[L_HS + (qr < 10 ? qr : 0) + 10 * k];
#pragma unroll
      for (int i = 0; i < 8; ++i) a8[i] = dot2(wqp[k][i], hp, a8[i]);
    }
    if (lane < 60) {
      *reinterpret_cast<float4*>(wred + qr * 48 + qc * 8) = float4{a8[0], a8[1], a8[2], a8[3]};
      *reinterpret_cast<float4*>(wred + qr * 48 + qc * 8 + 4) = float4{a8[4], a8[5], a8[6], a8[7]};
    }
    lds_wait();
    const int ql = lane < 48 ? lane : 47;
    float s0 = 0.f, s1 = 0.f;
#pragma unroll
    for (int g = 0; g < 5; ++g) { s0 += wred[g * 48 + ql]; s1 += wred[(g + 5) * 48 + ql]; }
    if (lane < 48) st16(rws, WS_P2 + (b * 192 + 48 * vw + lane) * 16, enc2(s0 + s1, tg));
    if (vw == 0) STAMP(13);
    u32x4 g[8];
    gather<8>(rws, WS_P2 + (h * 8 * 192 + 48 * vw + ql) * 16, 192 * 16, tg, g);
    const float qs = ((dec2(g[0]) + dec2(g[1])) + (dec2(g[2]) + dec2(g[3]))) + ((dec2(g[4]) + dec2(g[5])) + (dec2(g[6]) + dec2(g[7])));
    if (lane < 48) reinterpret_cast<u16*>(lds + L_Q)[48 * vw + lane] = (u16)bfbits(qs);
  }
  if (vw == 0) STAMP(14);
  bar();

  if (j == 4 || j == 5) {
    const int m2 = 2 * h + j - 4;
    const unsigned ob = lane < 50 ? 0u : OOB;
    u32x4 w[16]; unsigned cp2[8];
#pragma unroll
    for (int k = 0; k < 8; ++k) cp2[k] = __builtin_amdgcn_raw_buffer_load_b32(rc, (160 * vw + 2 * (mrg + 10 * k)) * 2 | ob, 0, 0);
#pragma unroll
    for (int k = 0; k < 16; ++k) w[k] = __builtin_amdgcn_raw_buffer_load_b128(rmod, ((160 * vw + 2 * (mrg + 10 * (k >> 1)) + (k & 1)) * (6 * D) + 2 * D + 40 * m2 + 8 * mcc) * 2 | ob, 0, AUX_NT);
    float a8[8];
#pragma unroll
    for (int i = 0; i < 8; ++i) a8[i] = 0.f;
#pragma unroll
    for (int k = 0; k < 8; ++k) {
      const unsigned sp = bfbits(silu_f(lo16(cp2[k]))) | (bfbits(silu_f(hi16(cp2[k]))) << 16);
#pragma unroll
      for (int i = 0; i < 4; ++i) { a8[2 * i] = dot2(plo(w[2 * k + 1][i], w[2 * k][i]), sp, a8[2 * i]); a8[2 * i + 1] = dot2(phi(w[2 * k + 1][i], w[2 * k][i]), sp, a8[2 * i + 1]); }
    }
    if (lane < 50) {
      *reinterpret_cast<float4*>(wred + mrg * 40 + mcc * 8) = float4{a8[0], a8[1], a8[2], a8[3]};
      *reinterpret_cast<float4*>(wred + mrg * 40 + mcc * 8 + 4) = float4{a8[4], a8[5], a8[6], a8[7]};
    }
    lds_wait();
    unsigned old = 0;
    if (lane == 0) old = __hip_atomic_fetch_add(lds + L_CNT + 1, 1u, __ATOMIC_RELAXED, __HIP_MEMORY_SCOPE_WORKGROUP);
    old = __builtin_amdgcn_readfirstlane(old);
    asm volatile("" ::: "memory");
    if (old == 3u) {
      float p4[4] = {0.f, 0.f, 0.f, 0.f};
#pragma unroll
      for (int g = 0; g < 10; ++g)
#pragma unroll
        for (int w4 = 0; w4 < 4; ++w4) p4[w4] += ldf[L_RED + w4 * 480 + g * 40 + gl];
      if (lane < 40) st8(rws, WS_M2 + (40 * m2 + lane) * 8, u32x2{bfbits((p4[0] + p4[1]) + (p4[2] + p4[3])) | tg.tl, tg.th});
      lds_wait();
      if (lane == 0) __hip_atomic_store(lds + L_CNT + 1, 8u, __ATOMIC_RELAXED, __HIP_MEMORY_SCOPE_WORKGROUP);
    }
    for (unsigned spin = 0; spin < (SPIN_LIMIT << 4); ++spin) {
      if (__hip_atomic_load(lds + L_CNT + 1, __ATOMIC_RELAXED, __HIP_MEMORY_SCOPE_WORKGROUP) >= 8u) break;
      asm volatile("s_sleep 2" ::: "memory");
    }
    asm volatile("" ::: "memory");
  }
  const int org = lane / 10, ocg = lane - 10 * org;
  unsigned wop[4][8];
  {
    const rsrc_t rwo = mk(wo, HD * D * 2);
    const unsigned o0 = lane < 60 ? ((h * DH + 48 * vw + 8 * org) * D + 80 * j + 8 * ocg) * 2 : OOB;
    u32x4 w[8];
#pragma unroll
    for (int k = 0; k < 8; ++k) w[k] = __builtin_amdgcn_raw_buffer_load_b128(rwo, o0, k * D * 2, AUX_NT);
#pragma unroll
    for (int p = 0; p < 4; ++p)
#pragma unroll
      for (int i = 0; i < 4; ++i) { wop[p][2 * i] = plo(w[2 * p + 1][i], w[2 * p][i]); wop[p][2 * i + 1] = phi(w[2 * p + 1][i], w[2 * p][i]); }
  }
  auto h1_stage = [&]() {
    bar();
    if (vw == 0) STAMP(4);
    {
      const float m0 = ldf[L_PART + 192], m1 = ldf[L_PART + 200 + 192], m2 = ldf[L_PART + 400 + 192], m3 = ldf[L_PART + 600 + 192];
      const float M = fmaxf(fmaxf(m0, m1), fmaxf(m2, m3));
      const float e0 = __expf(m0 - M), e1 = __expf(m1 - M), e2 = __expf(m2 - M), e3 = __expf(m3 - M);
      const int idx = vt < 192 ? vt : 193;
      float v = e0 * ldf[L_PART + idx];
      v = fmaf(e1, ldf[L_PART + 200 + idx], v); v = fmaf(e2, ldf[L_PART + 400 + idx], v); v = fmaf(e3, ldf[L_PART + 600 + idx], v);
      if (vt == 192) v = M;
      if (vt < 194) st16(rws, WS_R + (b * 200 + vt) * 16, enc2(v, tg));
    }
    {
      const int s = lane >> 3, dg = lane & 7;
      u32x4 g[8];
      const unsigned rb = WS_R + (h * 8 + s) * 200 * 16, r0 = rb + (48 * vw + 6 * dg) * 16;
      poll<8>([&](int k) { return __builtin_amdgcn_raw_buffer_load_b128(rws, k < 6 ? r0 + k * 16 : rb + (186 + k) * 16, 0, AUX_SC1); }, tg, g);
      if (vw == 0) STAMP(15);
      const float ms = dec2(g[6]), M = max_hi3(ms), e = __expf(ms - M);
      const float L = sum_hi3(e * dec2(g[7])), rl = __builtin_amdgcn_rcpf(L);
      float z[3];
#pragma unroll
      for (int k = 0; k < 3; ++k) z[k] = tr32(e * dec2(g[k]), e * dec2(g[k + 3]));
      const float z01 = tr16(z[0], z[1]), z2 = tr16(z[2], z[2]);
      const float ov = tr8(z01, z2, (lane & 8) != 0);
      u16* op = reinterpret_cast<u16*>(lds + L_OP + vw * 24) + 6 * dg + 3 * (lane >> 5);
      if ((lane & 24) != 24) op[(lane & 8) ? 2 : (lane >> 4) & 1] = (u16)bfbits(fdiv(ov, L, rl));
      lds_wait();
      const u32x4 opv = *reinterpret_cast<const u32x4*>(lds + L_OP + vw * 24 + 4 * (org < 6 ? org : 0));
      float y[8];
#pragma unroll
      for (int i = 0; i < 8; ++i) y[i] = 0.f;
#pragma unroll
      for (int p = 0; p < 4; ++p)
#pragma unroll
        for (int i = 0; i < 8; ++i) y[i] = dot2(wop[p][i], opv[p], y[i]);
      if (lane < 60) {
        float* dst = ldf + L_RED + (vw * 6 + org) * 80 + 8 * ocg;
        *reinterpret_cast<float4*>(dst) = float4{y[0], y[1], y[2], y[3]};
        *reinterpret_cast<float4*>(dst + 4) = float4{y[4], y[5], y[6], y[7]};
      }
    }
    bar();
    if (vw < 3) {
      const int yc = vt < 160 ? vt >> 1 : 79;
      const float* src = ldf + L_RED + (vt & 1) * 960 + yc;
      float p4[4] = {0.f, 0.f, 0.f, 0.f};
#pragma unroll
      for (int g = 0; g < 12; ++g) p4[g & 3] += src[g * 80];
      float y = (p4[0] + p4[1]) + (p4[2] + p4[3]);
      y += dpp<XOR1>(y);
      if (vt < 160 && (vt & 1) == 0) st16(rws, WS_Y + (((20 * j + (yc >> 2)) * 32 + h) * 4 + (yc & 3)) * 16, enc2(y, tg));
    }
    if (vw == 0) STAMP(5);
  };

  if (j == 2 || j == 3) {
    const int f = 2 * h + j - 2, rg = vt >> 3, cc = vt & 7;
    if (vw < 3) {
      const rsrc_t rwd = mk(wd, F * D * 2);
      const unsigned o0 = vt < 160 ? (24 * f * D + 4 * vt) * 2 : OOB;
      u32x2 w[24];
#pragma unroll
      for (int k = 0; k < 24; ++k) w[k] = __builtin_amdgcn_raw_buffer_load_b64(rwd, o0, k * D * 2, AUX_NT);
      if (vt < 160) {
#pragma unroll
        for (int p = 0; p < 12; ++p)
          *reinterpret_cast<u32x4*>(lds + L_WD + (p * 160 + vt) * 4) =
              u32x4{plo(w[2 * p + 1][0], w[2 * p][0]), phi(w[2 * p + 1][0], w[2 * p][0]), plo(w[2 * p + 1][1], w[2 * p][1]), phi(w[2 * p + 1][1], w[2 * p][1])};
      }
    }
    unsigned gp[10][8];
    {
      const rsrc_t rg_ = mk(wg, D * F * 2), ru_ = mk(wu, D * F * 2);
      const unsigned og = cc < 3 ? (20 * rg * F + 24 * f + 8 * cc) * 2 : OOB, ou = (cc >= 3 && cc < 6) ? (20 * rg * F + 24 * f + 8 * (cc - 3)) * 2 : OOB;
#pragma unroll
      for (int hf = 0; hf < 2; ++hf) {
        u32x4 w[10], w2[10];
#pragma unroll
        for (int k = 0; k < 10; ++k) {
          w[k] = __builtin_amdgcn_raw_buffer_load_b128(rg_, og, (10 * hf + k) * F * 2, AUX_NT);
          w2[k] = __builtin_amdgcn_raw_buffer_load_b128(ru_, ou, (10 * hf + k) * F * 2, AUX_NT);
        }
#pragma unroll
        for (int k = 0; k < 10; ++k) w[k] |= w2[k];
#pragma unroll
        for (int p = 0; p < 5; ++p)
#pragma unroll
          for (int i = 0; i < 4; ++i) { gp[5 * hf + p][2 * i] = plo(w[2 * p + 1][i], w[2 * p][i]); gp[5 * hf + p][2 * i + 1] = phi(w[2 * p + 1][i], w[2 * p][i]); }
      }
#pragma unroll
      for (int p = 0; p < 10; ++p)
#pragma unroll
        for (int i = 0; i < 8; ++i) asm volatile("" : "+v"(gp[p][i]));
    }
    unsigned po[5];
#pragma unroll
    for (int k = 0; k < 5; ++k) { const int p = 80 * vw + lane + 64 * k; po[k] = (unsigned)(p >= 320 ? p - 320 : p) * 16u; }
    float sbv[4], c1v[4];
    {
      u32x4 g[4];
      const unsigned off[4] = {WS_M2 + D * 8 + po[0], WS_M2 + D * 8 + po[1], WS_M2 + 2 * D * 8 + po[0], WS_M2 + 2 * D * 8 + po[1]};
      gather_slow<4>(rws, off, tg, g);
#pragma unroll
      for (int k = 0; k < 2; ++k) {
        const unsigned sp = decp(g[k]), cq = decp(g[2 + k]);
        sbv[2 * k] = lo16(sp); sbv[2 * k + 1] = hi16(sp); c1v[2 * k] = rbf(1.f + lo16(cq)); c1v[2 * k + 1] = rbf(1.f + hi16(cq));
      }
    }
    h1_stage();
    u32x4 wdr[12];
#pragma unroll
    for (int p = 0; p < 12; ++p) wdr[p] = *reinterpret_cast<const u32x4*>(lds + L_WD + (p * 160 + (vt < 160 ? vt : 0)) * 4);
    {
      u32x4 g[5];
      poll<5>([&](int k) { return __builtin_amdgcn_raw_buffer_load_b128(rws, WS_X1 + po[k], 0, AUX_SC1); }, tg, g);
      if (vw == 0) STAMP(6);
      float xv[10];
#pragma unroll
      for (int k = 0; k < 5; ++k) { xv[2 * k] = __uint_as_float(g[k][0] << 16); xv[2 * k + 1] = __uint_as_float(g[k][2] << 16); }
      const float s = ((xv[0] + xv[1]) + (xv[2] + xv[3])) + ((xv[4] + xv[5]) + (xv[6] + xv[7])) + (xv[8] + xv[9]);
      const float mean = sum_wave(s) * (1.f / D);
      float v0 = 0.f, v1 = 0.f;
#pragma unroll
      for (int k = 0; k < 5; ++k) { const float d0 = xv[2 * k] - mean, d1 = xv[2 * k + 1] - mean; v0 = fmaf(d0, d0, v0); v1 = fmaf(d1, d1, v1); }
      const float var = sum_wave(v0 + v1) * (1.f / D);
      const float mb2 = rbf(mean), den2 = rbf(sqrtf(rbf(rbf(var) + 1e-5f))), rd = __builtin_amdgcn_rcpf(den2);
#pragma unroll
      for (int k = 0; k < 2; ++k) {
        const float n0 = rbf(fdiv(rbf(xv[2 * k] - mb2), den2, rd)), n1 = rbf(fdiv(rbf(xv[2 * k + 1] - mb2), den2, rd));
        const float h0 = rbf(n0 * c1v[2 * k]) + sbv[2 * k], h1 = rbf(n1 * c1v[2 * k + 1]) + sbv[2 * k + 1];
        if (k == 0 || lane < 16) lds[L_H2 + vw * 80 + lane + 64 * k] = bfbits(h0) | (bfbits(h1) << 16);
      }
    }
    lds_wait();
    {
      float a8[8];
#pragma unroll
      for (int i = 0; i < 8; ++i) a8[i] = 0.f;
#pragma unroll
      for (int p2 = 0; p2 < 5; ++p2) {
        const u32x2 hp = *reinterpret_cast<const u32x2*>(lds + L_H2 + vw * 80 + 10 * (lane >> 3) + 2 * p2);
#pragma unroll
        for (int i = 0; i < 8; ++i) { a8[i] = dot2(gp[2 * p2][i], hp[0], a8[i]); a8[i] = dot2(gp[2 * p2 + 1][i], hp[1], a8[i]); }
      }
      float z1[4], z2[2];
#pragma unroll
      for (int k = 0; k < 4; ++k) z1[k] = tr32(a8[k], a8[k + 4]);
#pragma unroll
      for (int k = 0; k < 2; ++k) z2[k] = tr16(z1[k], z1[k + 2]);
      const float z3 = tr8(z2[0], z2[1], (lane & 8) != 0);
      ldf[L_GU + vw * 64 + cc * 8 + 4 * (lane >> 5) + 2 * ((lane >> 4) & 1) + ((lane >> 3) & 1)] = z3;
    }
    bar();
    if (vw < 3) {
      const int n = lane < 24 ? lane : 0;
      const float g = (ldf[L_GU + n] + ldf[L_GU + 64 + n]) + (ldf[L_GU + 128 + n] + ldf[L_GU + 192 + n]);
      const float u = (ldf[L_GU + 24 + n] + ldf[L_GU + 88 + n]) + (ldf[L_GU + 152 + n] + ldf[L_GU + 216 + n]);
      const unsigned ab = bfbits(rbf(silu_f(rbf(g))) * rbf(u));
      const unsigned nb = (unsigned)__builtin_amdgcn_mov_dpp((int)ab, XOR1, 0xf, 0xf, true);
      const unsigned ap = ab | (nb << 16);
      float y[4] = {0.f, 0.f, 0.f, 0.f};
#pragma unroll
      for (int p = 0; p < 12; ++p) {
        const unsigned a2 = (unsigned)__builtin_amdgcn_readlane((int)ap, 2 * p);
#pragma unroll
        for (int i = 0; i < 4; ++i) y[i] = dot2(wdr[p][i], a2, y[i]);
      }
      const bool o1 = (lane & 1) != 0, o2 = (lane & 2) != 0;
      { const float s0 = o1 ? y[0] : y[1], s1 = o1 ? y[2] : y[3]; const float r0 = dpp<XOR1>(s0), r1 = dpp<XOR1>(s1);
        if (o1) { y[0] = r0; y[2] = r1; } else { y[1] = r0; y[3] = r1; } }
      { const float s0 = o2 ? y[0] : y[2], s1 = o2 ? y[1] : y[3]; const float r0 = dpp<XOR2>(s0), r1 = dpp<XOR2>(s1);
        if (o2) { y[0] = r0; y[1] = r1; } else { y[2] = r0; y[3] = r1; } }
      if (vt < 160) {
#pragma unroll
        for (int i = 0; i < 4; ++i) st16(rws, WS_O + ((((vt & ~3) + i) * NFFN + f) * 4 + (vt & 3)) * 16, enc2(y[i], tg));
      }
    }
    if (vw == 0) STAMP(10);
  } else {
    const bool red = j != 4 && vw == 3;
    const int rr = 5 * h + (j < 2 ? j : j - 3), col = 4 * rr + (lane & 3);
    float ga = 0.f, gb = 0.f, xv = 0.f;
    if (red) {
      u32x4 g[2];
      const unsigned off[2] = {WS_M2 + (col & ~1) * 8, WS_M2 + (col & ~1) * 8 + 3 * D * 8};
      gather_slow<2>(rws, off, tg, g);
      const unsigned pa = decp(g[0]), pb = decp(g[1]);
      ga = (col & 1) ? hi16(pa) : lo16(pa); gb = (col & 1) ? hi16(pb) : lo16(pb);
      xv = __uint_as_float((unsigned)x[col] << 16);
    }
    h1_stage();
    if (red) {
      float x1;
      {
        u32x4 g[2];
        gather<2>(rws, WS_Y + (rr * 128 + lane) * 16, 64 * 16, tg, g);
        float y = dec2(g[0]) + dec2(g[1]);
        y += dpp<ROR4>(y); y = sum_hi3(y);
        x1 = rbf(xv + rbf(ga * rbf(y)));
        if (lane < 4) st8(rws, WS_X1 + col * 8, u32x2{bfbits(x1) | tg.tl, tg.th});
      }
      STAMP(3);
      {
        u32x4 g[4];
        gather<4>(rws, WS_O + (rr * NFFN * 4 + lane) * 16, 64 * 16, tg, g);
        float f = (dec2(g[0]) + dec2(g[1])) + (dec2(g[2]) + dec2(g[3]));
        f += dpp<ROR4>(f); f = sum_hi3(f);
        const float o = x1 + rbf(gb * rbf(f));
        if (lane < 4) out[col] = (u16)bfbits(o);
        if (b == 0 && lane == 0) *reinterpret_cast<u64*>(ws + WS_EPOCH) = E1;
      }
      STAMP(7);
    }
  }
}

extern "C" void kernel_launch(void* const* d_in, const int* in_sizes, int n_in, void* d_out, int out_size, void* d_ws, size_t ws_size, hipStream_t stream) {
  (void)in_sizes; (void)n_in; (void)out_size; (void)ws_size;
  k_block<<<NWG, 512, 0, stream>>>((const u16*)d_in[0], (const u16*)d_in[1], (const u16*)d_in[2], (const u16*)d_in[3], (const u16*)d_in[4],
                                   (const u16*)d_in[6], (const u16*)d_in[7], (const u16*)d_in[8], (const u16*)d_in[9], (const u16*)d_in[10],
                                   (u16*)d_out, (unsigned char*)d_ws);
}
```

```cpp
#include <hip/hip_runtime.h>
#include <cstdint>

typedef unsigned short u16;
typedef unsigned long long u64;
using u32x4 = __attribute__((ext_vector_type(4))) unsigned;
using u32x2 = __attribute__((ext_vector_type(2))) unsigned;
using bf16x2 = __attribute__((ext_vector_type(2))) __bf16;

#define KU 2
#define SPCT 75
#define PF 6
#define NL 2
#define PSLEEP 10
#define ROT 0
#define STAMPS 0

constexpr int D = 640, H = 32, SK = 32768, DH = 192, F = 1536, HD = H * DH;
constexpr int NWG = 256, NFFN = 64;
constexpr unsigned WS_EPOCH = 0;
constexpr unsigned WS_CTR = 1024;
constexpr unsigned WS_P1 = 16384;
constexpr unsigned WS_P2 = WS_P1 + 8 * 8 * 160 * 16;
constexpr unsigned WS_M2 = WS_P2 + 32 * 8 * 192 * 16;
constexpr unsigned WS_R = WS_M2 + 2560 * 8;
constexpr unsigned WS_Y = WS_R + 256 * 200 * 16;
constexpr unsigned WS_X1 = WS_Y + 160 * 32 * 64;
constexpr unsigned WS_O = WS_X1 + 640 * 8;
constexpr unsigned WS_ST = WS_O + 160 * NFFN * 64;
constexpr unsigned WS_END = WS_ST + 256 * 16 * 8;

__device__ __forceinline__ float rbf(float x) { return (float)(__bf16)x; }
__device__ __forceinline__ unsigned bfbits(float x) { return (unsigned)__builtin_bit_cast(u16, (__bf16)x); }
__device__ __forceinline__ float lo16(unsigned v) { return __uint_as_float(v << 16); }
__device__ __forceinline__ float hi16(unsigned v) { return __uint_as_float(v & 0xffff0000u); }
__device__ __forceinline__ float dot2(unsigned a, unsigned b, float acc) {
  return __builtin_amdgcn_fdot2_f32_bf16(__builtin_bit_cast(bf16x2, a), __builtin_bit_cast(bf16x2, b), acc, false);
}
template <int CTRL> __device__ __forceinline__ float dpp(float x) {
  return __builtin_bit_cast(float, __builtin_amdgcn_mov_dpp(__builtin_bit_cast(int, x), CTRL, 0xf, 0xf, true));
}
constexpr int XOR1 = 0xB1, XOR2 = 0x4E, HMIR = 0x141, ROR8 = 0x128, ROR4 = 0x124;
__device__ __forceinline__ float tr32(float a, float b) {
  auto s = __builtin_amdgcn_permlane32_swap(__float_as_uint(a), __float_as_uint(b), false, false);
  return __uint_as_float(s[0]) + __uint_as_float(s[1]);
}
__device__ __forceinline__ float tr16(float a, float b) {
  auto s = __builtin_amdgcn_permlane16_swap(__float_as_uint(a), __float_as_uint(b), false, false);
  return __uint_as_float(s[0]) + __uint_as_float(s[1]);
}
__device__ __forceinline__ float tr8(float a, float b, bool bit3) { const float t1 = a + dpp<ROR8>(a), t2 = b + dpp<ROR8>(b); return bit3 ? t2 : t1; }
__device__ __forceinline__ float mx32(float a) { auto s = __builtin_amdgcn_permlane32_swap(__float_as_uint(a), __float_as_uint(a), false, false); return fmaxf(__uint_as_float(s[0]), __uint_as_float(s[1])); }
__device__ __forceinline__ float mx16(float a) { auto s = __builtin_amdgcn_permlane16_swap(__float_as_uint(a), __float_as_uint(a), false, false); return fmaxf(__uint_as_float(s[0]), __uint_as_float(s[1])); }
__device__ __forceinline__ float sum_hi3(float a) { a += dpp<ROR8>(a); a = tr16(a, a); return tr32(a, a); }
__device__ __forceinline__ float max_hi3(float a) { a = fmaxf(a, dpp<ROR8>(a)); return mx32(mx16(a)); }
__device__ __forceinline__ float sum_row16(float a) { a += dpp<XOR1>(a); a += dpp<XOR2>(a); a += dpp<HMIR>(a); a += dpp<ROR8>(a); return a; }
__device__ __forceinline__ float sum_wave(float a) { a = sum_row16(a); a = tr16(a, a); return tr32(a, a); }
__device__ __forceinline__ unsigned plo(unsigned r1, unsigned r0) { return __builtin_amdgcn_perm(r1, r0, 0x05040100u); }
__device__ __forceinline__ unsigned phi(unsigned r1, unsigned r0) { return __builtin_amdgcn_perm(r1, r0, 0x07060302u); }
__device__ __forceinline__ float silu_f(float x) { return x / (1.f + __expf(-x)); }
__device__ __forceinline__ void bar() { asm volatile("s_waitcnt lgkmcnt(0)" ::: "memory"); __builtin_amdgcn_s_barrier(); asm volatile("" ::: "memory"); }

typedef __amdgpu_buffer_rsrc_t rsrc_t;
__device__ __forceinline__ rsrc_t mk(const void* p, unsigned bytes) { return __builtin_amdgcn_make_buffer_rsrc((void*)p, 0, bytes, 0x00020000); }
constexpr int AUX_NT = 2, AUX_SC1 = 16;

struct Tag { unsigned tl, th; };
__device__ __forceinline__ u32x4 enc2(float v, Tag t) { const unsigned b = __float_as_uint(v); return u32x4{(b & 0xffffu) | t.tl, t.th, (b >> 16) | t.tl, t.th}; }
__device__ __forceinline__ float dec2(u32x4 g) { return __uint_as_float((g[0] & 0xffffu) | (g[2] << 16)); }
__device__ __forceinline__ unsigned decp(u32x4 g) { return (g[0] & 0xffffu) | (g[2] << 16); }
__device__ __forceinline__ void st16(rsrc_t r, unsigned off, u32x4 v) { __builtin_amdgcn_raw_buffer_store_b128(v, r, off, 0, AUX_SC1); }
__device__ __forceinline__ void st8(rsrc_t r, unsigned off, u32x2 v) { __builtin_amdgcn_raw_buffer_store_b64(v, r, off, 0, AUX_SC1); }

constexpr unsigned SPIN_LIMIT = 1u << 19;
constexpr unsigned OOB = 0x80000000u;
__device__ __forceinline__ unsigned bad2(u32x4 g, Tag t) { return (((g[0] ^ t.tl) | (g[2] ^ t.tl)) >> 16) | (g[1] ^ t.th) | (g[3] ^ t.th); }
template <int N, class LD>
__device__ __forceinline__ void poll(LD ld, Tag t, u32x4 (&g)[N]) {
  u32x4 y[N];
#pragma unroll
  for (int k = 0; k < N; ++k) g[k] = ld(k);
  asm volatile("s_sleep %0" :: "i"(PSLEEP) : "memory");
  for (unsigned spin = 0; spin < SPIN_LIMIT; ++spin) {
#pragma unroll
    for (int k = 0; k < N; ++k) y[k] = ld(k);
    unsigned bad = 0;
#pragma unroll
    for (int k = 0; k < N; ++k) bad |= bad2(g[k], t);
    if (__builtin_amdgcn_ballot_w64(bad != 0u) == 0ull) break;
    asm volatile("" ::: "memory");
#pragma unroll
    for (int k = 0; k < N; ++k) g[k] = ld(k);
    bad = 0;
#pragma unroll
    for (int k = 0; k < N; ++k) bad |= bad2(y[k], t);
    if (__builtin_amdgcn_ballot_w64(bad != 0u) == 0ull) {
#pragma unroll
      for (int k = 0; k < N; ++k) g[k] = y[k];
      break;
    }
    asm volatile("" ::: "memory");
  }
}
template <int N>
__device__ __forceinline__ void gather(rsrc_t r, unsigned off, unsigned stride, Tag t, u32x4 (&g)[N]) {
  poll<N>([&](int k) { return __builtin_amdgcn_raw_buffer_load_b128(r, off + k * stride, 0, AUX_SC1); }, t, g);
}
template <int N>
__device__ __forceinline__ void gather_slow(rsrc_t r, const unsigned (&off)[N], Tag t, u32x4 (&g)[N]) {
  for (unsigned spin = 0; spin < SPIN_LIMIT; ++spin) {
#pragma unroll
    for (int k = 0; k < N; ++k) g[k] = __builtin_amdgcn_raw_buffer_load_b128(r, off[k], 0, AUX_SC1);
    unsigned bad = 0;
#pragma unroll
    for (int k = 0; k < N; ++k) bad |= bad2(g[k], t);
    if (__builtin_amdgcn_ballot_w64(bad != 0u) == 0ull) break;
    asm volatile("s_sleep 16" ::: "memory");
  }
}
__device__ __forceinline__ void lds_wait() { asm volatile("s_waitcnt lgkmcnt(0)" ::: "memory"); }
__device__ __forceinline__ float fdiv(float a, float d, float r) { const float q = a * r; return fmaf(fmaf(-q, d, a), r, q); }

#define STAMP(i) do { } while (0)

constexpr int L_RED = 0;
constexpr int L_HS = L_RED + 1920;
constexpr int L_Q = L_HS + 40;
constexpr int L_PART = L_Q + 96;
constexpr int L_OP = L_PART + 800;
constexpr int L_H2 = L_OP + 96;
constexpr int L_GU = L_H2 + 320;
constexpr int L_CNT = L_GU + 256;
constexpr int L_WD = L_CNT + 4;
constexpr int L_KV = L_WD + 7680;
constexpr int KVW = (NL > 0 ? NL : 1) * KU * 1536;
constexpr int L_TOTAL = L_KV + 4 * KVW < 24576 ? 24576 : L_KV + 4 * KVW;
static_assert(L_WD % 4 == 0 && L_KV % 4 == 0 && L_TOTAL * 4 <= 163840, "lds map");

__global__ void __launch_bounds__(512, 2)
k_block(const u16* __restrict__ x, const u16* __restrict__ Kc, const u16* __restrict__ Vc, const u16* __restrict__ wq, const u16* __restrict__ wo,
        const u16* __restrict__ wg, const u16* __restrict__ wu, const u16* __restrict__ wd, const u16* __restrict__ cnd, const u16* __restrict__ wmod,
        u16* __restrict__ out, unsigned char* __restrict__ ws) {
  __shared__ __attribute__((aligned(16))) unsigned lds[L_TOTAL];
  float* ldf = reinterpret_cast<float*>(lds);
  const int tid = threadIdx.x, lane = tid & 63, wv = __builtin_amdgcn_readfirstlane(tid >> 6);
  const int b = blockIdx.x, h = b >> 3, j = b & 7;
  const rsrc_t rws = mk(ws, WS_END);
  const rsrc_t rmod = mk(wmod, D * 6 * D * 2), rc = mk(cnd, D * 2);
  const int mrg = lane / 5, mcc = lane - 5 * mrg;
  const int gl = lane < 40 ? lane : 39;

  if (wv < 4) {
    constexpr int U = KU;
    constexpr int NW = 32;
    constexpr int TPG = SK / (8 * U);
    constexpr int S = (TPG / NW) * SPCT / 100;
    static_assert(S >= NL + 4, "queue");
    const int r = lane >> 3, c = lane & 7, rnk = j * 4 + wv;
    const unsigned loff = r * 384 + c * 16;
    const rsrc_t rk = mk((const char*)Kc + (size_t)h * SK * 384, SK * 384), rv = mk((const char*)Vc + (size_t)h * SK * 384, SK * 384);
    const unsigned* cp = (const unsigned*)(ws + WS_CTR + h * 256);
    if (j < 2) {
      const int j1 = (2 * h + j) & 7, kb = h >> 2;
      const unsigned col = (wv >> 1) * D + 80 * j1 + 40 * (wv & 1) + 8 * mcc;
      const unsigned ob = lane < 50 ? 0u : OOB;
      u32x4 wm[8]; unsigned cpair[4];
#pragma unroll
      for (int k = 0; k < 8; ++k) wm[k] = __builtin_amdgcn_raw_buffer_load_b128(rmod, ((80 * kb + 2 * (mrg + 10 * (k >> 1)) + (k & 1)) * (6 * D) + col) * 2 | ob, 0, AUX_NT);
      asm volatile("" ::: "memory");
#pragma unroll
      for (int k = 0; k < 4; ++k) cpair[k] = __builtin_amdgcn_raw_buffer_load_b32(rc, (80 * kb + 2 * (mrg + 10 * k)) * 2 | ob, 0, 0);
      const u32x2 ev = __builtin_amdgcn_raw_buffer_load_b64(rws, WS_EPOCH, 0, AUX_SC1);
      bar();
      float a8[8];
#pragma unroll
      for (int i = 0; i < 8; ++i) a8[i] = 0.f;
#pragma unroll
      for (int k = 0; k < 4; ++k) {
        const unsigned sp = bfbits(silu_f(lo16(cpair[k]))) | (bfbits(silu_f(hi16(cpair[k]))) << 16);
#pragma unroll
        for (int i = 0; i < 4; ++i) { a8[2 * i] = dot2(plo(wm[2 * k + 1][i], wm[2 * k][i]), sp, a8[2 * i]); a8[2 * i + 1] = dot2(phi(wm[2 * k + 1][i], wm[2 * k][i]), sp, a8[2 * i + 1]); }
      }
      float* wred = ldf + L_KV + wv * KVW;
      if (lane < 50) {
        *reinterpret_cast<float4*>(wred + mrg * 40 + mcc * 8) = float4{a8[0], a8[1], a8[2], a8[3]};
        *reinterpret_cast<float4*>(wred + mrg * 40 + mcc * 8 + 4) = float4{a8[4], a8[5], a8[6], a8[7]};
      }
      lds_wait();
      float s = 0.f;
#pragma unroll
      for (int g = 0; g < 10; ++g) s += wred[g * 40 + gl];
      const u64 E1 = (((u64)ev[1] << 32) | ev[0]) + 1ull;
      Tag tg; tg.tl = (unsigned)(E1 & 0xffffu) << 16; tg.th = (unsigned)(E1 >> 16);
      if (lane < 40) st16(rws, WS_P1 + ((j1 * 8 + kb) * 160 + 40 * wv + lane) * 16, enc2(s, tg));
      if (wv == 0) STAMP(11);
      asm volatile("s_waitcnt lgkmcnt(0)" ::: "memory");
    } else {
      bar();
    }
    u32x4 A[2 * U][3], B[2 * U][3];
#define TOFF(tile) ((unsigned)(ROT ? ((tile) + h * ROT) & (TPG - 1) : (tile)) * (3072u * U))
#define LOADT(X, tile) do { const unsigned so_ = TOFF(tile), vo_ = (tile) < TPG ? loff : OOB; \
    _Pragma("unroll") for (int u = 0; u < 2 * U; ++u) _Pragma("unroll") for (int q_ = 0; q_ < 3; ++q_) \
      X[u][q_] = __builtin_amdgcn_raw_buffer_load_b128(u < U ? rk : rv, vo_ + (u % U) * 3072 + q_ * 128, so_, AUX_NT); } while (0)
#define LOADP(X, tile, i0) do { const unsigned so_ = TOFF(tile); \
    _Pragma("unroll") for (int u = 0; u < 2 * U; ++u) _Pragma("unroll") for (int q_ = 0; q_ < 3; ++q_) { \
      if ((i0) + u * 3 + q_ >= PF) asm volatile("s_waitcnt vmcnt(%0)" :: "i"(PF - 1) : "memory"); \
      X[u][q_] = __builtin_amdgcn_raw_buffer_load_b128(u < U ? rk : rv, loff + (u % U) * 3072 + q_ * 128, so_, AUX_NT); } } while (0)
#define USE(X) do { float s_[U]; float mn_ = m; \
    _Pragma("unroll") for (int u = 0; u < U; ++u) { float d_ = 0.f; \
      _Pragma("unroll") for (int q_ = 0; q_ < 3; ++q_) _Pragma("unroll") for (int i = 0; i < 4; ++i) d_ = dot2(X[u][q_][i], qv[q_][i], d_); \
      d_ += dpp<XOR1>(d_); d_ += dpp<XOR2>(d_); d_ += dpp<HMIR>(d_); \
      s_[u] = rbf(rbf(d_) * 0.07216878364870322f); mn_ = fmaxf(mn_, s_[u]); } \
    const float al_ = __expf(m - mn_); m = mn_; l *= al_; \
    _Pragma("unroll") for (int i = 0; i < 24; ++i) acc[i] *= al_; \
    _Pragma("unroll") for (int u = 0; u < U; ++u) { const float p_ = __expf(s_[u] - mn_); l += p_; \
      _Pragma("unroll") for (int q_ = 0; q_ < 3; ++q_) _Pragma("unroll") for (int i = 0; i < 4; ++i) { \
        acc[q_ * 8 + 2 * i] = fmaf(p_, lo16(X[U + u][q_][i]), acc[q_ * 8 + 2 * i]); acc[q_ * 8 + 2 * i + 1] = fmaf(p_, hi16(X[U + u][q_][i]), acc[q_ * 8 + 2 * i + 1]); } } } while (0)
#define GRAB(gg) do { u64 sv_; asm volatile("s_mov_b64 %1, exec\n\ts_mov_b64 exec, 1\n\tglobal_atomic_add %0, %2, %3, %4 sc0\n\ts_mov_b64 exec, %1" : "=&v"(gg), "=&s"(sv_) : "v"(0u), "v"(1u), "s"(cp) : "memory"); } while (0)
    LOADP(B, rnk, 0);
    LOADP(A, NW + rnk, 6 * U);
    const unsigned lbase = (unsigned)(L_KV + wv * KVW) * 4u;
#pragma unroll
    for (int t = 0; t < NL; ++t) {
      const unsigned so_ = TOFF((2 + t) * NW + rnk);
#pragma unroll
      for (int u = 0; u < 2 * U; ++u)
#pragma unroll
        for (int q_ = 0; q_ < 3; ++q_) {
          unsigned keep;
          asm volatile("s_waitcnt vmcnt(%6)\n\ts_mov_b32 %0, m0\n\ts_mov_b32 m0, %1\n\ts_nop 4\n\tbuffer_load_dwordx4 %2, %3, %4 offen nt lds\n\ts_mov_b32 m0, %0"
                       : "=&s"(keep) : "s"(lbase + (unsigned)((t * 2 * U + u) * 3 + q_) * 1024u), "v"(loff), "s"(u < U ? rk : rv),
                         "s"(so_ + (unsigned)((u % U) * 3072 + q_ * 128)), "i"(0), "i"(PF - 1) : "memory");
        }
    }
    if (wv == 0) STAMP(8);
    bar();
    u32x4 qv[3];
#pragma unroll
    for (int q_ = 0; q_ < 3; ++q_) qv[q_] = *reinterpret_cast<const u32x4*>(lds + L_Q + q_ * 32 + c * 4);
    if (wv == 0) STAMP(1);
    float m = -INFINITY, l = 0.f, acc[24];
#pragma unroll
    for (int i = 0; i < 24; ++i) acc[i] = 0.f;
    int tA = 0, tB, n = 2 + NL;
    unsigned gA = 0, gB = 0;
    do {
      USE(B);
      asm volatile("s_waitcnt vmcnt(%1)" : "+v"(gB) : "i"(6 * U) : "memory");
      tB = tA >= TPG ? TPG : n >= S ? S * NW + (int)__builtin_amdgcn_readfirstlane(gB) : n * NW + rnk; ++n;
      if (n >= S && tB < TPG) GRAB(gA);
      LOADT(B, tB);
      asm volatile("" ::: "memory");
      if (tA < TPG) USE(A);
      asm volatile("s_waitcnt vmcnt(%1)" : "+v"(gA) : "i"(6 * U) : "memory");
      tA = tB >= TPG ? TPG : n >= S ? S * NW + (int)__builtin_amdgcn_readfirstlane(gA) : n * NW + rnk; ++n;
      if (n >= S && tA < TPG) GRAB(gB);
      LOADT(A, tA);
      asm volatile("" ::: "memory");
      if (NL > 0 && n == 4 + NL) {
        asm volatile("s_waitcnt vmcnt(%0)" :: "i"(12 * U) : "memory");
#pragma unroll 1
        for (int t = 0; t < NL; ++t) {
          u32x4 C[2 * U][3];
#pragma unroll
          for (int u = 0; u < 2 * U; ++u)
#pragma unroll
            for (int q_ = 0; q_ < 3; ++q_) C[u][q_] = *reinterpret_cast<const u32x4*>(lds + L_KV + wv * KVW + ((t * 2 * U + u) * 3 + q_) * 256 + lane * 4);
          USE(C);
        }
      }
    } while (tB < TPG);
    const float M = max_hi3(m);
    const float sc = __expf(m - M);
    const float L = sum_hi3(l * sc);
    float y1[12], y2[6], y3[3];
#pragma unroll
    for (int k = 0; k < 12; ++k) y1[k] = tr32(acc[k] * sc, acc[k + 12] * sc);
#pragma unroll
    for (int k = 0; k < 6; ++k) y2[k] = tr16(y1[k], y1[k + 6]);
#pragma unroll
    for (int k = 0; k < 3; ++k) y3[k] = tr8(y2[k], y2[k + 3], (lane & 8) != 0);
    const int ai0 = 12 * (lane >> 5) + 6 * ((lane >> 4) & 1) + 3 * ((lane >> 3) & 1);
#pragma unroll
    for (int k = 0; k < 3; ++k) { const int ai = ai0 + k; ldf[L_PART + wv * 200 + (ai >> 3) * 64 + c * 8 + (ai & 7)] = y3[k]; }
    if (lane == 0) { ldf[L_PART + wv * 200 + 192] = M; ldf[L_PART + wv * 200 + 193] = L; }
    if (wv == 0) STAMP(2);
    bar();
    return;
  }

  const int vt = tid - 256, vw = wv - 4;
  if (vw == 0) STAMP(0);
  float* wred = ldf + L_RED + vw * 480;
  const u32x2 ev = __builtin_amdgcn_raw_buffer_load_b64(rws, WS_EPOCH, 0, AUX_SC1);
  unsigned xp[5];
#pragma unroll
  for (int k = 0; k < 5; ++k) xp[k] = *reinterpret_cast<const unsigned*>(x + 2 * (lane + 64 * k));
  const float xi = __uint_as_float((unsigned)x[80 * j + 20 * vw + (gl >> 1)] << 16);
  const int qr = lane / 6, qc = lane - 6 * qr;
  unsigned wqp[4][8];
  {
    const rsrc_t rwq = mk(wq, D * HD * 2);
    const unsigned ob = lane < 60 ? 0u : OOB;
    u32x4 wqr[8];
#pragma unroll
    for (int k = 0; k < 8; ++k) wqr[k] = __builtin_amdgcn_raw_buffer_load_b128(rwq, ((80 * j + 2 * (qr + 10 * (k >> 1)) + (k & 1)) * HD + h * DH + 48 * vw + 8 * qc) * 2 | ob, 0, AUX_NT);
    if (j == 0 && vt == 0) (void)__hip_atomic_exchange((unsigned*)(ws + WS_CTR + h * 256), 0u, __ATOMIC_RELAXED, __HIP_MEMORY_SCOPE_AGENT);
    if (vt == 255) { lds[L_CNT] = 0u; lds[L_CNT + 1] = 0u; }
    bar();
    if (vw == 0) STAMP(9);
#pragma unroll
    for (int k = 0; k < 4; ++k)
#pragma unroll
      for (int i = 0; i < 4; ++i) { wqp[k][2 * i] = plo(wqr[2 * k + 1][i], wqr[2 * k][i]); wqp[k][2 * i + 1] = phi(wqr[2 * k + 1][i], wqr[2 * k][i]); }
#pragma unroll
    for (int k = 0; k < 4; ++k)
#pragma unroll
      for (int i = 0; i < 8; ++i) asm volatile("" : "+v"(wqp[k][i]));
  }
  const u64 E1 = (((u64)ev[1] << 32) | ev[0]) + 1ull;
  Tag tg; tg.tl = (unsigned)(E1 & 0xffffu) << 16; tg.th = (unsigned)(E1 >> 16);
  float nrm;
  {
    float s = 0.f;
#pragma unroll
    for (int k = 0; k < 5; ++k) s += lo16(xp[k]) + hi16(xp[k]);
    const float mean = sum_wave(s) * (1.f / D);
    float v = 0.f;
#pragma unroll
    for (int k = 0; k < 5; ++k) { const float d0 = lo16(xp[k]) - mean, d1 = hi16(xp[k]) - mean; v = fmaf(d0, d0, v); v = fmaf(d1, d1, v); }
    const float var = sum_wave(v) * (1.f / D);
    const float mb = rbf(mean), den = rbf(sqrtf(rbf(rbf(var) + 1e-5f)));
    nrm = rbf(rbf(xi - mb) / den);
    asm volatile("s_waitcnt vmcnt(0)" : "+v"(nrm) :: "memory");
  }
  {
    u32x4 g[8];
    gather<8>(rws, WS_P1 + (j * 8 * 160 + 20 * vw + (gl >> 1) + 80 * (gl & 1)) * 16, 160 * 16, tg, g);
    const float sa = ((dec2(g[0]) + dec2(g[1])) + (dec2(g[2]) + dec2(g[3]))) + ((dec2(g[4]) + dec2(g[5])) + (dec2(g[6]) + dec2(g[7])));
    const float ca = dpp<XOR1>(sa);
    const float hv = rbf(nrm * rbf(1.f + rbf(ca))) + rbf(sa);
    if (lane < 40 && (lane & 1) == 0) reinterpret_cast<u16*>(lds + L_HS)[20 * vw + (lane >> 1)] = (u16)bfbits(hv);
    lds_wait();
    if (lane == 0) (void)__hip_atomic_fetch_add(lds + L_CNT, 1u, __ATOMIC_RELAXED, __HIP_MEMORY_SCOPE_WORKGROUP);
    if (vw == 0) STAMP(12);
    for (unsigned spin = 0; spin < (SPIN_LIMIT << 4); ++spin)
      if (__hip_atomic_load(lds + L_CNT, __ATOMIC_RELAXED, __HIP_MEMORY_SCOPE_WORKGROUP) >= 4u) break;
    asm volatile("" ::: "memory");
  }
  {
    float a8[8];
#pragma unroll
    for (int i = 0; i < 8; ++i) a8[i] = 0.f;
#pragma unroll
    for (int k = 0; k < 4; ++k) {
      const unsigned hp = lds[L_HS + (qr < 10 ? qr : 0) + 10 * k];
#pragma unroll
      for (int i = 0; i < 8; ++i) a8[i] = dot2(wqp[k][i], hp, a8[i]);
    }
    if (lane < 60) {
      *reinterpret_cast<float4*>(wred + qr * 48 + qc * 8) = float4{a8[0], a8[1], a8[2], a8[3]};
      *reinterpret_cast<float4*>(wred + qr * 48 + qc * 8 + 4) = float4{a8[4], a8[5], a8[6], a8[7]};
    }
    lds_wait();
    const int ql = lane < 48 ? lane : 47;
    float s0 = 0.f, s1 = 0.f;
#pragma unroll
    for (int g = 0; g < 5; ++g) { s0 += wred[g * 48 + ql]; s1 += wred[(g + 5) * 48 + ql]; }
    if (lane < 48) st16(rws, WS_P2 + (b * 192 + 48 * vw + lane) * 16, enc2(s0 + s1, tg));
    if (vw == 0) STAMP(13);
    u32x4 g[8];
    gather<8>(rws, WS_P2 + (h * 8 * 192 + 48 * vw + ql) * 16, 192 * 16, tg, g);
    const float qs = ((dec2(g[0]) + dec2(g[1])) + (dec2(g[2]) + dec2(g[3]))) + ((dec2(g[4]) + dec2(g[5])) + (dec2(g[6]) + dec2(g[7])));
    if (lane < 48) reinterpret_cast<u16*>(lds + L_Q)[48 * vw + lane] = (u16)bfbits(qs);
  }
  if (vw == 0) STAMP(14);
  bar();

  if (j == 4 || j == 5) {
    const int m2 = 2 * h + j - 4;
    const unsigned ob = lane < 50 ? 0u : OOB;
    u32x4 w[16]; unsigned cp2[8];
#pragma unroll
    for (int k = 0; k < 8; ++k) cp2[k] = __builtin_amdgcn_raw_buffer_load_b32(rc, (160 * vw + 2 * (mrg + 10 * k)) * 2 | ob, 0, 0);
#pragma unroll
    for (int k = 0; k < 16; ++k) w[k] = __builtin_amdgcn_raw_buffer_load_b128(rmod, ((160 * vw + 2 * (mrg + 10 * (k >> 1)) + (k & 1)) * (6 * D) + 2 * D + 40 * m2 + 8 * mcc) * 2 | ob, 0, AUX_NT);
    float a8[8];
#pragma unroll
    for (int i = 0; i < 8; ++i) a8[i] = 0.f;
#pragma unroll
    for (int k = 0; k < 8; ++k) {
      const unsigned sp = bfbits(silu_f(lo16(cp2[k]))) | (bfbits(silu_f(hi16(cp2[k]))) << 16);
#pragma unroll
      for (int i = 0; i < 4; ++i) { a8[2 * i] = dot2(plo(w[2 * k + 1][i], w[2 * k][i]), sp, a8[2 * i]); a8[2 * i + 1] = dot2(phi(w[2 * k + 1][i], w[2 * k][i]), sp, a8[2 * i + 1]); }
    }
    if (lane < 50) {
      *reinterpret_cast<float4*>(wred + mrg * 40 + mcc * 8) = float4{a8[0], a8[1], a8[2], a8[3]};
      *reinterpret_cast<float4*>(wred + mrg * 40 + mcc * 8 + 4) = float4{a8[4], a8[5], a8[6], a8[7]};
    }
    lds_wait();
    unsigned old = 0;
    if (lane == 0) old = __hip_atomic_fetch_add(lds + L_CNT + 1, 1u, __ATOMIC_RELAXED, __HIP_MEMORY_SCOPE_WORKGROUP);
    old = __builtin_amdgcn_readfirstlane(old);
    asm volatile("" ::: "memory");
    if (old == 3u) {
      float p4[4] = {0.f, 0.f, 0.f, 0.f};
#pragma unroll
      for (int g = 0; g < 10; ++g)
#pragma unroll
        for (int w4 = 0; w4 < 4; ++w4) p4[w4] += ldf[L_RED + w4 * 480 + g * 40 + gl];
      if (lane < 40) st8(rws, WS_M2 + (40 * m2 + lane) * 8, u32x2{bfbits((p4[0] + p4[1]) + (p4[2] + p4[3])) | tg.tl, tg.th});
      lds_wait();
      if (lane == 0) __hip_atomic_store(lds + L_CNT + 1, 8u, __ATOMIC_RELAXED, __HIP_MEMORY_SCOPE_WORKGROUP);
    }
    for (unsigned spin = 0; spin < (SPIN_LIMIT << 4); ++spin) {
      if (__hip_atomic_load(lds + L_CNT + 1, __ATOMIC_RELAXED, __HIP_MEMORY_SCOPE_WORKGROUP) >= 8u) break;
      asm volatile("s_sleep 2" ::: "memory");
    }
    asm volatile("" ::: "memory");
  }
  const int org = lane / 10, ocg = lane - 10 * org;
  unsigned wop[4][8];
  {
    const rsrc_t rwo = mk(wo, HD * D * 2);
    const unsigned o0 = lane < 60 ? ((h * DH + 48 * vw + 8 * org) * D + 80 * j + 8 * ocg) * 2 : OOB;
    u32x4 w[8];
#pragma unroll
    for (int k = 0; k < 8; ++k) w[k] = __builtin_amdgcn_raw_buffer_load_b128(rwo, o0, k * D * 2, AUX_NT);
#pragma unroll
    for (int p = 0; p < 4; ++p)
#pragma unroll
      for (int i = 0; i < 4; ++i) { wop[p][2 * i] = plo(w[2 * p + 1][i], w[2 * p][i]); wop[p][2 * i + 1] = phi(w[2 * p + 1][i], w[2 * p][i]); }
  }
  auto h1_stage = [&]() {
    bar();
    if (vw == 0) STAMP(4);
    {
      const float m0 = ldf[L_PART + 192], m1 = ldf[L_PART + 200 + 192], m2 = ldf[L_PART + 400 + 192], m3 = ldf[L_PART + 600 + 192];
      const float M = fmaxf(fmaxf(m0, m1), fmaxf(m2, m3));
      const float e0 = __expf(m0 - M), e1 = __expf(m1 - M), e2 = __expf(m2 - M), e3 = __expf(m3 - M);
      const int idx = vt < 192 ? vt : 193;
      float v = e0 * ldf[L_PART + idx];
      v = fmaf(e1, ldf[L_PART + 200 + idx], v); v = fmaf(e2, ldf[L_PART + 400 + idx], v); v = fmaf(e3, ldf[L_PART + 600 + idx], v);
      if (vt == 192) v = M;
      if (vt < 194) st16(rws, WS_R + (b * 200 + vt) * 16, enc2(v, tg));
    }
    {
      const int s = lane >> 3, dg = lane & 7;
      u32x4 g[8];
      const unsigned rb = WS_R + (h * 8 + s) * 200 * 16, r0 = rb + (48 * vw + dg) * 16;
      poll<8>([&](int k) { return __builtin_amdgcn_raw_buffer_load_b128(rws, k < 6 ? r0 + k * 128 : rb + (186 + k) * 16, 0, AUX_SC1); }, tg, g);
      if (vw == 0) STAMP(15);
      const float ms = dec2(g[6]), M = max_hi3(ms), e = __expf(ms - M);
      const float L = sum_hi3(e * dec2(g[7])), rl = __builtin_amdgcn_rcpf(L);
      float z[3];
#pragma unroll
      for (int k = 0; k < 3; ++k) z[k] = tr32(e * dec2(g[k]), e * dec2(g[k + 3]));
      const float z01 = tr16(z[0], z[1]), z2 = tr16(z[2], z[2]);
      const float ov = tr8(z01, z2, (lane & 8) != 0);
      u16* op = reinterpret_cast<u16*>(lds + L_OP + vw * 24) + dg + 24 * (lane >> 5);
      if ((lane & 24) != 24) op[(lane & 8) ? 16 : 8 * ((lane >> 4) & 1)] = (u16)bfbits(fdiv(ov, L, rl));
      lds_wait();
      const u32x4 opv = *reinterpret_cast<const u32x4*>(lds + L_OP + vw * 24 + 4 * (org < 6 ? org : 0));
      float y[8];
#pragma unroll
      for (int i = 0; i < 8; ++i) y[i] = 0.f;
#pragma unroll
      for (int p = 0; p < 4; ++p)
#pragma unroll
        for (int i = 0; i < 8; ++i) y[i] = dot2(wop[p][i], opv[p], y[i]);
      if (lane < 60) {
        float* dst = ldf + L_RED + (vw * 6 + org) * 80 + 8 * ocg;
        *reinterpret_cast<float4*>(dst) = float4{y[0], y[1], y[2], y[3]};
        *reinterpret_cast<float4*>(dst + 4) = float4{y[4], y[5], y[6], y[7]};
      }
    }
    bar();
    if (vw < 3) {
      const int yc = vt < 160 ? vt >> 1 : 79;
      const float* src = ldf + L_RED + (vt & 1) * 960 + yc;
      float p4[4] = {0.f, 0.f, 0.f, 0.f};
#pragma unroll
      for (int g = 0; g < 12; ++g) p4[g & 3] += src[g * 80];
      float y = (p4[0] + p4[1]) + (p4[2] + p4[3]);
      y += dpp<XOR1>(y);
      if (vt < 160 && (vt & 1) == 0) st16(rws, WS_Y + (((20 * j + (yc >> 2)) * 32 + h) * 4 + (yc & 3)) * 16, enc2(y, tg));
    }
    if (vw == 0) STAMP(5);
  };

  if (j == 2 || j == 3) {
    const int f = 2 * h + j - 2, rg = vt >> 3, cc = vt & 7;
    if (vw < 3) {
      const rsrc_t rwd = mk(wd, F * D * 2);
      const unsigned o0 = vt < 160 ? (24 * f * D + 4 * vt) * 2 : OOB;
      u32x2 w[24];
#pragma unroll
      for (int k = 0; k < 24; ++k) w[k] = __builtin_amdgcn_raw_buffer_load_b64(rwd, o0, k * D * 2, AUX_NT);
      if (vt < 160) {
#pragma unroll
        for (int p = 0; p < 12; ++p)
          *reinterpret_cast<u32x4*>(lds + L_WD + (p * 160 + vt) * 4) =
              u32x4{plo(w[2 * p + 1][0], w[2 * p][0]), phi(w[2 * p + 1][0], w[2 * p][0]), plo(w[2 * p + 1][1], w[2 * p][1]), phi(w[2 * p + 1][1], w[2 * p][1])};
      }
    }
    unsigned gp[10][8];
    {
      const rsrc_t rg_ = mk(wg, D * F * 2), ru_ = mk(wu, D * F * 2);
      const unsigned og = cc < 3 ? (20 * rg * F + 24 * f + 8 * cc) * 2 : OOB, ou = (cc >= 3 && cc < 6) ? (20 * rg * F + 24 * f + 8 * (cc - 3)) * 2 : OOB;
#pragma unroll
      for (int hf = 0; hf < 2; ++hf) {
        u32x4 w[10], w2[10];
#pragma unroll
        for (int k = 0; k < 10; ++k) {
          w[k] = __builtin_amdgcn_raw_buffer_load_b128(rg_, og, (10 * hf + k) * F * 2, AUX_NT);
          w2[k] = __builtin_amdgcn_raw_buffer_load_b128(ru_, ou, (10 * hf + k) * F * 2, AUX_NT);
        }
#pragma unroll
        for (int k = 0; k < 10; ++k) w[k] |= w2[k];
#pragma unroll
        for (int p = 0; p < 5; ++p)
#pragma unroll
          for (int i = 0; i < 4; ++i) { gp[5 * hf + p][2 * i] = plo(w[2 * p + 1][i], w[2 * p][i]); gp[5 * hf + p][2 * i + 1] = phi(w[2 * p + 1][i], w[2 * p][i]); }
      }
#pragma unroll
      for (int p = 0; p < 10; ++p)
#pragma unroll
        for (int i = 0; i < 8; ++i) asm volatile("" : "+v"(gp[p][i]));
    }
    unsigned po[5];
#pragma unroll
    for (int k = 0; k < 5; ++k) { const int p = 80 * vw + lane + 64 * k; po[k] = (unsigned)(p >= 320 ? p - 320 : p) * 16u; }
    float sbv[4], c1v[4];
    {
      u32x4 g[4];
      const unsigned off[4] = {WS_M2 + D * 8 + po[0], WS_M2 + D * 8 + po[1], WS_M2 + 2 * D * 8 + po[0], WS_M2 + 2 * D * 8 + po[1]};
      gather_slow<4>(rws, off, tg, g);
#pragma unroll
      for (int k = 0; k < 2; ++k) {
        const unsigned sp = decp(g[k]), cq = decp(g[2 + k]);
        sbv[2 * k] = lo16(sp); sbv[2 * k + 1] = hi16(sp); c1v[2 * k] = rbf(1.f + lo16(cq)); c1v[2 * k + 1] = rbf(1.f + hi16(cq));
      }
    }
    h1_stage();
    u32x4 wdr[12];
#pragma unroll
    for (int p = 0; p < 12; ++p) wdr[p] = *reinterpret_cast<const u32x4*>(lds + L_WD + (p * 160 + (vt < 160 ? vt : 0)) * 4);
    {
      u32x4 g[5];
      poll<5>([&](int k) { return __builtin_amdgcn_raw_buffer_load_b128(rws, WS_X1 + po[k], 0, AUX_SC1); }, tg, g);
      if (vw == 0) STAMP(6);
      float xv[10];
#pragma unroll
      for (int k = 0; k < 5; ++k) { xv[2 * k] = __uint_as_float(g[k][0] << 16); xv[2 * k + 1] = __uint_as_float(g[k][2] << 16); }
      const float s = ((xv[0] + xv[1]) + (xv[2] + xv[3])) + ((xv[4] + xv[5]) + (xv[6] + xv[7])) + (xv[8] + xv[9]);
      const float mean = sum_wave(s) * (1.f / D);
      float v0 = 0.f, v1 = 0.f;
#pragma unroll
      for (int k = 0; k < 5; ++k) { const float d0 = xv[2 * k] - mean, d1 = xv[2 * k + 1] - mean; v0 = fmaf(d0, d0, v0); v1 = fmaf(d1, d1, v1); }
      const float var = sum_wave(v0 + v1) * (1.f / D);
      const float mb2 = rbf(mean), den2 = rbf(sqrtf(rbf(rbf(var) + 1e-5f))), rd = __builtin_amdgcn_rcpf(den2);
#pragma unroll
      for (int k = 0; k < 2; ++k) {
        const float n0 = rbf(fdiv(rbf(xv[2 * k] - mb2), den2, rd)), n1 = rbf(fdiv(rbf(xv[2 * k + 1] - mb2), den2, rd));
        const float h0 = rbf(n0 * c1v[2 * k]) + sbv[2 * k], h1 = rbf(n1 * c1v[2 * k + 1]) + sbv[2 * k + 1];
        if (k == 0 || lane < 16) lds[L_H2 + vw * 80 + lane + 64 * k] = bfbits(h0) | (bfbits(h1) << 16);
      }
    }
    lds_wait();
    {
      float a8[8];
#pragma unroll
      for (int i = 0; i < 8; ++i) a8[i] = 0.f;
#pragma unroll
      for (int p2 = 0; p2 < 5; ++p2) {
        const u32x2 hp = *reinterpret_cast<const u32x2*>(lds + L_H2 + vw * 80 + 10 * (lane >> 3) + 2 * p2);
#pragma unroll
        for (int i = 0; i < 8; ++i) { a8[i] = dot2(gp[2 * p2][i], hp[0], a8[i]); a8[i] = dot2(gp[2 * p2 + 1][i], hp[1], a8[i]); }
      }
      float z1[4], z2[2];
#pragma unroll
      for (int k = 0; k < 4; ++k) z1[k] = tr32(a8[k], a8[k + 4]);
#pragma unroll
      for (int k = 0; k < 2; ++k) z2[k] = tr16(z1[k], z1[k + 2]);
      const float z3 = tr8(z2[0], z2[1], (lane & 8) != 0);
      ldf[L_GU + vw * 64 + cc * 8 + 4 * (lane >> 5) + 2 * ((lane >> 4) & 1) + ((lane >> 3) & 1)] = z3;
    }
    bar();
    if (vw < 3) {
      const int n = lane < 24 ? lane : 0;
      const float g = (ldf[L_GU + n] + ldf[L_GU + 64 + n]) + (ldf[L_GU + 128 + n] + ldf[L_GU + 192 + n]);
      const float u = (ldf[L_GU + 24 + n] + ldf[L_GU + 88 + n]) + (ldf[L_GU + 152 + n] + ldf[L_GU + 216 + n]);
      const unsigned ab = bfbits(rbf(silu_f(rbf(g))) * rbf(u));
      const unsigned nb = (unsigned)__builtin_amdgcn_mov_dpp((int)ab, XOR1, 0xf, 0xf, true);
      const unsigned ap = ab | (nb << 16);
      float y[4] = {0.f, 0.f, 0.f, 0.f};
#pragma unroll
      for (int p = 0; p < 12; ++p) {
        const unsigned a2 = (unsigned)__builtin_amdgcn_readlane((int)ap, 2 * p);
#pragma unroll
        for (int i = 0; i < 4; ++i) y[i] = dot2(wdr[p][i], a2, y[i]);
      }
      const bool o1 = (lane & 1) != 0, o2 = (lane & 2) != 0;
      { const float s0 = o1 ? y[0] : y[1], s1 = o1 ? y[2] : y[3]; const float r0 = dpp<XOR1>(s0), r1 = dpp<XOR1>(s1);
        if (o1) { y[0] = r0; y[2] = r1; } else { y[1] = r0; y[3] = r1; } }
      { const float s0 = o2 ? y[0] : y[2], s1 = o2 ? y[1] : y[3]; const float r0 = dpp<XOR2>(s0), r1 = dpp<XOR2>(s1);
        if (o2) { y[0] = r0; y[1] = r1; } else { y[2] = r0; y[3] = r1; } }
      if (vt < 160) {
#pragma unroll
        for (int i = 0; i < 4; ++i) st16(rws, WS_O + ((((vt & ~3) + i) * NFFN + f) * 4 + (vt & 3)) * 16, enc2(y[i], tg));
      }
    }
    if (vw == 0) STAMP(10);
  } else {
    const bool red = j != 4 && vw == 3;
    const int rr = 5 * h + (j < 2 ? j : j - 3), col = 4 * rr + (lane & 3);
    float ga = 0.f, gb = 0.f, xv = 0.f;
    if (red) {
      u32x4 g[2];
      const unsigned off[2] = {WS_M2 + (col & ~1) * 8, WS_M2 + (col & ~1) * 8 + 3 * D * 8};
      gather_slow<2>(rws, off, tg, g);
      const unsigned pa = decp(g[0]), pb = decp(g[1]);
      ga = (col & 1) ? hi16(pa) : lo16(pa); gb = (col & 1) ? hi16(pb) : lo16(pb);
      xv = __uint_as_float((unsigned)x[col] << 16);
    }
    h1_stage();
    if (red) {
      float x1;
      {
        u32x4 g[2];
        gather<2>(rws, WS_Y + (rr * 128 + lane) * 16, 64 * 16, tg, g);
        float y = dec2(g[0]) + dec2(g[1]);
        y += dpp<ROR4>(y); y = sum_hi3(y);
        x1 = rbf(xv + rbf(ga * rbf(y)));
        if (lane < 4) st8(rws, WS_X1 + col * 8, u32x2{bfbits(x1) | tg.tl, tg.th});
      }
      STAMP(3);
      {
        u32x4 g[4];
        gather<4>(rws, WS_O + (rr * NFFN * 4 + lane) * 16, 64 * 16, tg, g);
        float f = (dec2(g[0]) + dec2(g[1])) + (dec2(g[2]) + dec2(g[3]));
        f += dpp<ROR4>(f); f = sum_hi3(f);
        const float o = x1 + rbf(gb * rbf(f));
        if (lane < 4) out[col] = (u16)bfbits(o);
        if (b == 0 && lane == 0) *reinterpret_cast<u64*>(ws + WS_EPOCH) = E1;
      }
      STAMP(7);
    }
  }
}

extern "C" void kernel_launch(void* const* d_in, const int* in_sizes, int n_in, void* d_out, int out_size, void* d_ws, size_t ws_size, hipStream_t stream) {
  (void)in_sizes; (void)n_in; (void)out_size; (void)ws_size;
  k_block<<<NWG, 512, 0, stream>>>((const u16*)d_in[0], (const u16*)d_in[1], (const u16*)d_in[2], (const u16*)d_in[3], (const u16*)d_in[4],
                                   (const u16*)d_in[6], (const u16*)d_in[7], (const u16*)d_in[8], (const u16*)d_in[9], (const u16*)d_in[10],
                                   (u16*)d_out, (unsigned char*)d_ws);
}
```

```cpp
#include <hip/hip_runtime.h>
#include <cstdint>

typedef unsigned short u16;
typedef unsigned long long u64;
using u32x4 = __attribute__((ext_vector_type(4))) unsigned;
using u32x2 = __attribute__((ext_vector_type(2))) unsigned;
using bf16x2 = __attribute__((ext_vector_type(2))) __bf16;

#define KU 2
#define SPCT 75
#define PF 12
#define NL 2
#define PSLEEP 10
#define ROT 0
#define DLY 0
#define STAMPS 0

constexpr int D = 640, H = 32, SK = 32768, DH = 192, F = 1536, HD = H * DH;
constexpr int NWG = 256, NFFN = 64;
constexpr unsigned WS_EPOCH = 0;
constexpr unsigned WS_CTR = 1024;
constexpr unsigned WS_P1 = 16384;
constexpr unsigned WS_P2 = WS_P1 + 2 * 1280 * 16;
constexpr unsigned WS_M2 = WS_P2 + 32 * 2 * 192 * 16;
constexpr unsigned WS_R = WS_M2 + 2560 * 8;
constexpr unsigned WS_Y = WS_R + 256 * 200 * 16;
constexpr unsigned WS_X1 = WS_Y + 160 * 32 * 64;
constexpr unsigned WS_O = WS_X1 + 640 * 8;
constexpr unsigned WS_ST = WS_O + 160 * NFFN * 64;
constexpr unsigned WS_END = WS_ST + 256 * 16 * 8;

__device__ __forceinline__ float rbf(float x) { return (float)(__bf16)x; }
__device__ __forceinline__ unsigned bfbits(float x) { return (unsigned)__builtin_bit_cast(u16, (__bf16)x); }
__device__ __forceinline__ float lo16(unsigned v) { return __uint_as_float(v << 16); }
__device__ __forceinline__ float hi16(unsigned v) { return __uint_as_float(v & 0xffff0000u); }
__device__ __forceinline__ float dot2(unsigned a, unsigned b, float acc) {
  return __builtin_amdgcn_fdot2_f32_bf16(__builtin_bit_cast(bf16x2, a), __builtin_bit_cast(bf16x2, b), acc, false);
}
template <int CTRL> __device__ __forceinline__ float dpp(float x) {
  return __builtin_bit_cast(float, __builtin_amdgcn_mov_dpp(__builtin_bit_cast(int, x), CTRL, 0xf, 0xf, true));
}
constexpr int XOR1 = 0xB1, XOR2 = 0x4E, HMIR = 0x141, ROR8 = 0x128, ROR4 = 0x124;
__device__ __forceinline__ float tr32(float a, float b) {
  auto s = __builtin_amdgcn_permlane32_swap(__float_as_uint(a), __float_as_uint(b), false, false);
  return __uint_as_float(s[0]) + __uint_as_float(s[1]);
}
__device__ __forceinline__ float tr16(float a, float b) {
  auto s = __builtin_amdgcn_permlane16_swap(__float_as_uint(a), __float_as_uint(b), false, false);
  return __uint_as_float(s[0]) + __uint_as_float(s[1]);
}
__device__ __forceinline__ float tr8(float a, float b, bool bit3) { const float t1 = a + dpp<ROR8>(a), t2 = b + dpp<ROR8>(b); return bit3 ? t2 : t1; }
__device__ __forceinline__ float mx32(float a) { auto s = __builtin_amdgcn_permlane32_swap(__float_as_uint(a), __float_as_uint(a), false, false); return fmaxf(__uint_as_float(s[0]), __uint_as_float(s[1])); }
__device__ __forceinline__ float mx16(float a) { auto s = __builtin_amdgcn_permlane16_swap(__float_as_uint(a), __float_as_uint(a), false, false); return fmaxf(__uint_as_float(s[0]), __uint_as_float(s[1])); }
__device__ __forceinline__ float sum_hi3(float a) { a += dpp<ROR8>(a); a = tr16(a, a); return tr32(a, a); }
__device__ __forceinline__ float max_hi3(float a) { a = fmaxf(a, dpp<ROR8>(a)); return mx32(mx16(a)); }
__device__ __forceinline__ float sum_row16(float a) { a += dpp<XOR1>(a); a += dpp<XOR2>(a); a += dpp<HMIR>(a); a += dpp<ROR8>(a); return a; }
__device__ __forceinline__ float sum_wave(float a) { a = sum_row16(a); a = tr16(a, a); return tr32(a, a); }
__device__ __forceinline__ unsigned plo(unsigned r1, unsigned r0) { return __builtin_amdgcn_perm(r1, r0, 0x05040100u); }
__device__ __forceinline__ unsigned phi(unsigned r1, unsigned r0) { return __builtin_amdgcn_perm(r1, r0, 0x07060302u); }
__device__ __forceinline__ float silu_f(float x) { return x / (1.f + __expf(-x)); }
__device__ __forceinline__ void bar() { asm volatile("s_waitcnt lgkmcnt(0)" ::: "memory"); __builtin_amdgcn_s_barrier(); asm volatile("" ::: "memory"); }

typedef __amdgpu_buffer_rsrc_t rsrc_t;
__device__ __forceinline__ rsrc_t mk(const void* p, unsigned bytes) { return __builtin_amdgcn_make_buffer_rsrc((void*)p, 0, bytes, 0x00020000); }
constexpr int AUX_NT = 2, AUX_SC1 = 16;

struct Tag { unsigned tl, th; };
__device__ __forceinline__ u32x4 enc2(float v, Tag t) { const unsigned b = __float_as_uint(v); return u32x4{(b & 0xffffu) | t.tl, t.th, (b >> 16) | t.tl, t.th}; }
__device__ __forceinline__ float dec2(u32x4 g) { return __uint_as_float((g[0] & 0xffffu) | (g[2] << 16)); }
__device__ __forceinline__ unsigned decp(u32x4 g) { return (g[0] & 0xffffu) | (g[2] << 16); }
__device__ __forceinline__ void st16(rsrc_t r, unsigned off, u32x4 v) { __builtin_amdgcn_raw_buffer_store_b128(v, r, off, 0, AUX_SC1); }
__device__ __forceinline__ void st8(rsrc_t r, unsigned off, u32x2 v) { __builtin_amdgcn_raw_buffer_store_b64(v, r, off, 0, AUX_SC1); }

constexpr unsigned SPIN_LIMIT = 1u << 19;
constexpr unsigned OOB = 0x80000000u;
__device__ __forceinline__ unsigned bad2(u32x4 g, Tag t) { return (((g[0] ^ t.tl) | (g[2] ^ t.tl)) >> 16) | (g[1] ^ t.th) | (g[3] ^ t.th); }
template <int N, class LD>
__device__ __forceinline__ void poll(LD ld, Tag t, u32x4 (&g)[N]) {
  u32x4 y[N];
#pragma unroll
  for (int k = 0; k < N; ++k) g[k] = ld(k);
  asm volatile("s_sleep %0" :: "i"(PSLEEP) : "memory");
  for (unsigned spin = 0; spin < SPIN_LIMIT; ++spin) {
#pragma unroll
    for (int k = 0; k < N; ++k) y[k] = ld(k);
    unsigned bad = 0;
#pragma unroll
    for (int k = 0; k < N; ++k) bad |= bad2(g[k], t);
    if (__builtin_amdgcn_ballot_w64(bad != 0u) == 0ull) break;
    asm volatile("" ::: "memory");
#pragma unroll
    for (int k = 0; k < N; ++k) g[k] = ld(k);
    bad = 0;
#pragma unroll
    for (int k = 0; k < N; ++k) bad |= bad2(y[k], t);
    if (__builtin_amdgcn_ballot_w64(bad != 0u) == 0ull) {
#pragma unroll
      for (int k = 0; k < N; ++k) g[k] = y[k];
      break;
    }
    asm volatile("" ::: "memory");
  }
}
template <int N>
__device__ __forceinline__ void gather(rsrc_t r, unsigned off, unsigned stride, Tag t, u32x4 (&g)[N]) {
  poll<N>([&](int k) { return __builtin_amdgcn_raw_buffer_load_b128(r, off + k * stride, 0, AUX_SC1); }, t, g);
}
template <int N>
__device__ __forceinline__ void gather_slow(rsrc_t r, const unsigned (&off)[N], Tag t, u32x4 (&g)[N]) {
  for (unsigned spin = 0; spin < SPIN_LIMIT; ++spin) {
#pragma unroll
    for (int k = 0; k < N; ++k) g[k] = __builtin_amdgcn_raw_buffer_load_b128(r, off[k], 0, AUX_SC1);
    unsigned bad = 0;
#pragma unroll
    for (int k = 0; k < N; ++k) bad |= bad2(g[k], t);
    if (__builtin_amdgcn_ballot_w64(bad != 0u) == 0ull) break;
    asm volatile("s_sleep 16" ::: "memory");
  }
}
__device__ __forceinline__ void lds_wait() { asm volatile("s_waitcnt lgkmcnt(0)" ::: "memory"); }
__device__ __forceinline__ float fdiv(float a, float d, float r) { const float q = a * r; return fmaf(fmaf(-q, d, a), r, q); }

#define STAMP(i) do { } while (0)

constexpr int L_RED = 0;
constexpr int L_HS = L_RED + 1920;
constexpr int L_Q = L_HS + 160;
constexpr int L_PART = L_Q + 96;
constexpr int L_OP = L_PART + 800;
constexpr int L_H2 = L_OP + 96;
constexpr int L_GU = L_H2 + 320;
constexpr int L_CNT = L_GU + 256;
constexpr int L_WD = L_CNT + 4;
constexpr int L_KV = L_WD + 7680;
constexpr int KVW = (NL > 0 ? NL : 1) * KU * 1536;
constexpr int L_TOTAL = L_KV + 4 * KVW < 24576 ? 24576 : L_KV + 4 * KVW;
static_assert(L_WD % 4 == 0 && L_KV % 4 == 0 && L_TOTAL * 4 <= 163840, "lds map");

__global__ void __launch_bounds__(512, 2)
k_block(const u16* __restrict__ x, const u16* __restrict__ Kc, const u16* __restrict__ Vc, const u16* __restrict__ wq, const u16* __restrict__ wo,
        const u16* __restrict__ wg, const u16* __restrict__ wu, const u16* __restrict__ wd, const u16* __restrict__ cnd, const u16* __restrict__ wmod,
        u16* __restrict__ out, unsigned char* __restrict__ ws) {
  __shared__ __attribute__((aligned(16))) unsigned lds[L_TOTAL];
  float* ldf = reinterpret_cast<float*>(lds);
  const int tid = threadIdx.x, lane = tid & 63, wv = __builtin_amdgcn_readfirstlane(tid >> 6);
  const int b = blockIdx.x, h = b >> 3, j = b & 7;
  const rsrc_t rws = mk(ws, WS_END);
  const rsrc_t rmod = mk(wmod, D * 6 * D * 2), rc = mk(cnd, D * 2);
  const int mrg = lane / 5, mcc = lane - 5 * mrg;
  const int gl = lane < 40 ? lane : 39;
  if (wv == 4) STAMP(0);
  const u32x2 ev = __builtin_amdgcn_raw_buffer_load_b64(rws, WS_EPOCH, 0, AUX_SC1);
  if (tid == 511) { lds[L_CNT] = 0u; lds[L_CNT + 1] = 0u; }

  if (j == 2 || j == 3) {
    const int kb = j - 2;
    u32x4 wm[4]; unsigned cpair[2];
    const unsigned ob = lane < 50 ? 0u : OOB;
#pragma unroll
    for (int k = 0; k < 4; ++k) wm[k] = __builtin_amdgcn_raw_buffer_load_b128(rmod, ((320 * kb + 40 * wv + 2 * (mrg + 10 * (k >> 1)) + (k & 1)) * (6 * D) + 40 * h + 8 * mcc) * 2 | ob, 0, AUX_NT);
#pragma unroll
    for (int k = 0; k < 2; ++k) cpair[k] = __builtin_amdgcn_raw_buffer_load_b32(rc, (320 * kb + 40 * wv + 2 * (mrg + 10 * k)) * 2 | ob, 0, 0);
    float a8[8];
#pragma unroll
    for (int i = 0; i < 8; ++i) a8[i] = 0.f;
#pragma unroll
    for (int k = 0; k < 2; ++k) {
      const unsigned sp = bfbits(silu_f(lo16(cpair[k]))) | (bfbits(silu_f(hi16(cpair[k]))) << 16);
#pragma unroll
      for (int i = 0; i < 4; ++i) { a8[2 * i] = dot2(plo(wm[2 * k + 1][i], wm[2 * k][i]), sp, a8[2 * i]); a8[2 * i + 1] = dot2(phi(wm[2 * k + 1][i], wm[2 * k][i]), sp, a8[2 * i + 1]); }
    }
    if (lane < 50) {
      float* d_ = ldf + L_WD + (wv * 10 + mrg) * 40 + mcc * 8;
      *reinterpret_cast<float4*>(d_) = float4{a8[0], a8[1], a8[2], a8[3]};
      *reinterpret_cast<float4*>(d_ + 4) = float4{a8[4], a8[5], a8[6], a8[7]};
    }
    const u64 E1 = (((u64)ev[1] << 32) | ev[0]) + 1ull;
    Tag tg; tg.tl = (unsigned)(E1 & 0xffffu) << 16; tg.th = (unsigned)(E1 >> 16);
    bar();
    if (wv >= 3) {
      const int t = tid - 192;
      const float* src = ldf + L_WD + (t & 7) * 400 + (t >> 3);
      float s0 = 0.f, s1 = 0.f;
#pragma unroll
      for (int g = 0; g < 5; ++g) { s0 += src[g * 40]; s1 += src[(g + 5) * 40]; }
      float s = s0 + s1;
      s += dpp<XOR1>(s); s += dpp<XOR2>(s); s += dpp<HMIR>(s);
      if ((t & 7) == 0) st16(rws, WS_P1 + (kb * 1280 + 40 * h + (t >> 3)) * 16, enc2(s, tg));
      if (wv == 4) STAMP(11);
    }
    bar();
  } else if (j < 2) {
    unsigned xp[5];
    if (j == 0 && tid == 0) (void)__hip_atomic_exchange((unsigned*)(ws + WS_CTR + h * 256), 0u, __ATOMIC_RELAXED, __HIP_MEMORY_SCOPE_AGENT);
#pragma unroll
    for (int k = 0; k < 5; ++k) xp[k] = *reinterpret_cast<const unsigned*>(x + 2 * (lane + 64 * k));
    const int hr = tid < 320 ? tid : 319;
    const float xi = __uint_as_float((unsigned)x[320 * j + hr] << 16);
    asm volatile("" ::: "memory");
    const int qr = lane / 12, qc = lane - 12 * qr;
    u32x4 wqr[16];
    {
      const rsrc_t rwq = mk(wq, D * HD * 2);
      const unsigned o0 = lane < 60 ? ((320 * j + 80 * (wv >> 1) + 2 * qr) * HD + h * DH + 96 * (wv & 1) + 8 * qc) * 2 : OOB;
#pragma unroll
      for (int k = 0; k < 16; ++k) wqr[k] = __builtin_amdgcn_raw_buffer_load_b128(rwq, o0, (10 * (k >> 1) + (k & 1)) * HD * 2, AUX_NT);
    }
    asm volatile("" ::: "memory");
    const u64 E1 = (((u64)ev[1] << 32) | ev[0]) + 1ull;
    Tag tg; tg.tl = (unsigned)(E1 & 0xffffu) << 16; tg.th = (unsigned)(E1 >> 16);
    if (wv < 5) {
      float sx = 0.f;
#pragma unroll
      for (int k = 0; k < 5; ++k) sx += lo16(xp[k]) + hi16(xp[k]);
      const float mean = sum_wave(sx) * (1.f / D);
      float v = 0.f;
#pragma unroll
      for (int k = 0; k < 5; ++k) { const float d0 = lo16(xp[k]) - mean, d1 = hi16(xp[k]) - mean; v = fmaf(d0, d0, v); v = fmaf(d1, d1, v); }
      const float var = sum_wave(v) * (1.f / D);
      const float mb = rbf(mean), den = rbf(sqrtf(rbf(rbf(var) + 1e-5f)));
      float nrm = rbf(rbf(xi - mb) / den);
      asm volatile("" : "+v"(nrm));
      u32x4 g[4];
      const unsigned p0 = WS_P1 + (320 * j + tid) * 16;
      poll<4>([&](int k) { return __builtin_amdgcn_raw_buffer_load_b128(rws, p0 + (k & 1) * 1280 * 16 + (k >> 1) * 640 * 16, 0, AUX_SC1); }, tg, g);
      const float sa = dec2(g[0]) + dec2(g[1]), ca = dec2(g[2]) + dec2(g[3]);
      const float hv = rbf(nrm * rbf(1.f + rbf(ca))) + rbf(sa);
      reinterpret_cast<u16*>(lds + L_HS)[tid] = (u16)bfbits(hv);
      if (wv == 0) STAMP(12);
    }
    unsigned wqp[8][8];
#pragma unroll
    for (int k = 0; k < 8; ++k)
#pragma unroll
      for (int i = 0; i < 4; ++i) { wqp[k][2 * i] = plo(wqr[2 * k + 1][i], wqr[2 * k][i]); wqp[k][2 * i + 1] = phi(wqr[2 * k + 1][i], wqr[2 * k][i]); }
    asm volatile("s_waitcnt vmcnt(0)" ::: "memory");
    bar();
    {
      float a8[8];
#pragma unroll
      for (int i = 0; i < 8; ++i) a8[i] = 0.f;
#pragma unroll
      for (int k = 0; k < 8; ++k) {
        const unsigned hp = lds[L_HS + 40 * (wv >> 1) + (qr < 5 ? qr : 0) + 5 * k];
#pragma unroll
        for (int i = 0; i < 8; ++i) a8[i] = dot2(wqp[k][i], hp, a8[i]);
      }
      if (lane < 60) {
        float* d_ = ldf + L_WD + 3200 + ((wv >> 1) * 5 + qr) * 192 + 96 * (wv & 1) + qc * 8;
        *reinterpret_cast<float4*>(d_) = float4{a8[0], a8[1], a8[2], a8[3]};
        *reinterpret_cast<float4*>(d_ + 4) = float4{a8[4], a8[5], a8[6], a8[7]};
      }
    }
    bar();
    if (wv >= 2) {
      const int t = tid - 128;
      const float* src = ldf + L_WD + 3200 + (t & 1) * 1920 + (t >> 1);
      float s0 = 0.f, s1 = 0.f;
#pragma unroll
      for (int g = 0; g < 5; ++g) { s0 += src[g * 192]; s1 += src[(g + 5) * 192]; }
      float s = s0 + s1;
      s += dpp<XOR1>(s);
      if ((t & 1) == 0) st16(rws, WS_P2 + ((h * 2 + j) * 192 + (t >> 1)) * 16, enc2(s, tg));
      if (wv == 4) STAMP(13);
    }
    bar();
  }

  if (wv < 4) {
    constexpr int U = KU;
    constexpr int NW = 32;
    constexpr int TPG = SK / (8 * U);
    constexpr int S = (TPG / NW) * SPCT / 100;
    static_assert(S >= NL + 8, "queue");
    const int r = lane >> 3, c = lane & 7, rnk = j * 4 + wv;
    const unsigned loff = r * 384 + c * 16;
    const rsrc_t rk = mk((const char*)Kc + (size_t)h * SK * 384, SK * 384), rv = mk((const char*)Vc + (size_t)h * SK * 384, SK * 384);
    const unsigned* cp = (const unsigned*)(ws + WS_CTR + h * 256);
    u32x4 A[2 * U][3], B[2 * U][3], C[2 * U][3];
#define TOFF(tile) ((unsigned)(ROT ? ((tile) + h * ROT) & (TPG - 1) : (tile)) * (3072u * U))
#define LOADT(X, tile) do { const unsigned so_ = TOFF(tile), vo_ = (tile) < TPG ? loff : OOB; \
    _Pragma("unroll") for (int u = 0; u < 2 * U; ++u) _Pragma("unroll") for (int q_ = 0; q_ < 3; ++q_) \
      X[u][q_] = __builtin_amdgcn_raw_buffer_load_b128(u < U ? rk : rv, vo_ + (u % U) * 3072 + q_ * 128, so_, AUX_NT); } while (0)
#define LOADP(X, tile, i0) do { const unsigned so_ = TOFF(tile); \
    _Pragma("unroll") for (int u = 0; u < 2 * U; ++u) _Pragma("unroll") for (int q_ = 0; q_ < 3; ++q_) { \
      if ((i0) + u * 3 + q_ >= PF) asm volatile("s_waitcnt vmcnt(%0)" :: "i"(PF > 63 ? 63 : PF - 1) : "memory"); \
      X[u][q_] = __builtin_amdgcn_raw_buffer_load_b128(u < U ? rk : rv, loff + (u % U) * 3072 + q_ * 128, so_, AUX_NT); } } while (0)
#define USE(X) do { float s_[U]; float mn_ = m; \
    _Pragma("unroll") for (int u = 0; u < U; ++u) { float d_ = 0.f; \
      _Pragma("unroll") for (int q_ = 0; q_ < 3; ++q_) _Pragma("unroll") for (int i = 0; i < 4; ++i) d_ = dot2(X[u][q_][i], qv[q_][i], d_); \
      d_ += dpp<XOR1>(d_); d_ += dpp<XOR2>(d_); d_ += dpp<HMIR>(d_); \
      s_[u] = rbf(rbf(d_) * 0.07216878364870322f); mn_ = fmaxf(mn_, s_[u]); } \
    const float al_ = __expf(m - mn_); m = mn_; l *= al_; \
    _Pragma("unroll") for (int i = 0; i < 24; ++i) acc[i] *= al_; \
    _Pragma("unroll") for (int u = 0; u < U; ++u) { const float p_ = __expf(s_[u] - mn_); l += p_; \
      _Pragma("unroll") for (int q_ = 0; q_ < 3; ++q_) _Pragma("unroll") for (int i = 0; i < 4; ++i) { \
        acc[q_ * 8 + 2 * i] = fmaf(p_, lo16(X[U + u][q_][i]), acc[q_ * 8 + 2 * i]); acc[q_ * 8 + 2 * i + 1] = fmaf(p_, hi16(X[U + u][q_][i]), acc[q_ * 8 + 2 * i + 1]); } } } while (0)
#define GRAB(gg) do { u64 sv_; asm volatile("s_mov_b64 %1, exec\n\ts_mov_b64 exec, 1\n\tglobal_atomic_add %0, %2, %3, %4 sc0\n\ts_mov_b64 exec, %1" : "=&v"(gg), "=&s"(sv_) : "v"(0u), "v"(1u), "s"(cp) : "memory"); } while (0)
    if (j >= 4 && DLY > 0) asm volatile("s_sleep %0" :: "i"(DLY) : "memory");
    LOADP(A, rnk, 0);
    if (j < 2) bar();
    LOADP(B, NW + rnk, 6 * U);
    LOADP(C, 2 * NW + rnk, 12 * U);
    const unsigned lbase = (unsigned)(L_KV + wv * KVW) * 4u;
#pragma unroll
    for (int t = 0; t < NL; ++t) {
      const unsigned so_ = TOFF((3 + t) * NW + rnk);
#pragma unroll
      for (int u = 0; u < 2 * U; ++u)
#pragma unroll
        for (int q_ = 0; q_ < 3; ++q_) {
          unsigned keep;
          asm volatile("s_waitcnt vmcnt(%6)\n\ts_mov_b32 %0, m0\n\ts_mov_b32 m0, %1\n\ts_nop 4\n\tbuffer_load_dwordx4 %2, %3, %4 offen nt lds\n\ts_mov_b32 m0, %0"
                       : "=&s"(keep) : "s"(lbase + (unsigned)((t * 2 * U + u) * 3 + q_) * 1024u), "v"(loff), "s"(u < U ? rk : rv),
                         "s"(so_ + (unsigned)((u % U) * 3072 + q_ * 128)), "i"(0), "i"(PF > 63 ? 63 : PF - 1) : "memory");
        }
    }
    if (wv == 0) STAMP(8);
    if (j >= 2) bar();
    u32x4 qv[3];
#pragma unroll
    for (int q_ = 0; q_ < 3; ++q_) qv[q_] = *reinterpret_cast<const u32x4*>(lds + L_Q + q_ * 32 + c * 4);
    if (wv == 0) STAMP(1);
    float m = -INFINITY, l = 0.f, acc[24];
#pragma unroll
    for (int i = 0; i < 24; ++i) acc[i] = 0.f;
    int tA = 0, tB = 0, tC = 0, n = 3 + NL;
    unsigned gA = 0, gB = 0, gC = 0;
#define NEXT(X, tX, gX, tP, gN) do { \
      asm volatile("s_waitcnt vmcnt(%1)" : "+v"(gX) : "i"(12 * U) : "memory"); \
      tX = tP >= TPG ? TPG : n >= S ? S * NW + (int)__builtin_amdgcn_readfirstlane(gX) : n * NW + rnk; ++n; \
      if (n + 1 >= S && tX < TPG) GRAB(gN); \
      LOADT(X, tX); \
      asm volatile("" ::: "memory"); } while (0)
    do {
      if (tA < TPG) USE(A);
      NEXT(A, tA, gA, tB, gC);
      if (tB < TPG) USE(B);
      NEXT(B, tB, gB, tC, gA);
      if (tC < TPG) USE(C);
      if (NL > 0 && n == 5 + NL) {
        asm volatile("s_waitcnt vmcnt(%0)" :: "i"(12 * U) : "memory");
#pragma unroll 1
        for (int t = 0; t < NL; ++t) {
#pragma unroll
          for (int u = 0; u < 2 * U; ++u)
#pragma unroll
            for (int q_ = 0; q_ < 3; ++q_) C[u][q_] = *reinterpret_cast<const u32x4*>(lds + L_KV + wv * KVW + ((t * 2 * U + u) * 3 + q_) * 256 + lane * 4);
          USE(C);
        }
      }
      NEXT(C, tC, gC, tA, gB);
    } while (tA < TPG || tB < TPG || tC < TPG);
    const float M = max_hi3(m);
    const float sc = __expf(m - M);
    const float L = sum_hi3(l * sc);
    float y1[12], y2[6], y3[3];
#pragma unroll
    for (int k = 0; k < 12; ++k) y1[k] = tr32(acc[k] * sc, acc[k + 12] * sc);
#pragma unroll
    for (int k = 0; k < 6; ++k) y2[k] = tr16(y1[k], y1[k + 6]);
#pragma unroll
    for (int k = 0; k < 3; ++k) y3[k] = tr8(y2[k], y2[k + 3], (lane & 8) != 0);
    const int ai0 = 12 * (lane >> 5) + 6 * ((lane >> 4) & 1) + 3 * ((lane >> 3) & 1);
#pragma unroll
    for (int k = 0; k < 3; ++k) { const int ai = ai0 + k; ldf[L_PART + wv * 200 + (ai >> 3) * 64 + c * 8 + (ai & 7)] = y3[k]; }
    if (lane == 0) { ldf[L_PART + wv * 200 + 192] = M; ldf[L_PART + wv * 200 + 193] = L; }
    if (wv == 0) STAMP(2);
    bar();
    return;
  }

  const int vt = tid - 256, vw = wv - 4;
  float* wred = ldf + L_RED + vw * 480;
  const u64 E1 = (((u64)ev[1] << 32) | ev[0]) + 1ull;
  Tag tg; tg.tl = (unsigned)(E1 & 0xffffu) << 16; tg.th = (unsigned)(E1 >> 16);
  {
    const int ql = lane < 48 ? lane : 47;
    u32x4 g[2];
    gather<2>(rws, WS_P2 + (h * 2 * 192 + 48 * vw + ql) * 16, 192 * 16, tg, g);
    if (lane < 48) reinterpret_cast<u16*>(lds + L_Q)[48 * vw + lane] = (u16)bfbits(dec2(g[0]) + dec2(g[1]));
  }
  if (vw == 0) STAMP(14);
  bar();

  if (j == 4 || j == 5) {
    const int m2 = 2 * h + j - 4;
    const unsigned ob = lane < 50 ? 0u : OOB;
    u32x4 w[16]; unsigned cp2[8];
#pragma unroll
    for (int k = 0; k < 8; ++k) cp2[k] = __builtin_amdgcn_raw_buffer_load_b32(rc, (160 * vw + 2 * (mrg + 10 * k)) * 2 | ob, 0, 0);
#pragma unroll
    for (int k = 0; k < 16; ++k) w[k] = __builtin_amdgcn_raw_buffer_load_b128(rmod, ((160 * vw + 2 * (mrg + 10 * (k >> 1)) + (k & 1)) * (6 * D) + 2 * D + 40 * m2 + 8 * mcc) * 2 | ob, 0, AUX_NT);
    float a8[8];
#pragma unroll
    for (int i = 0; i < 8; ++i) a8[i] = 0.f;
#pragma unroll
    for (int k = 0; k < 8; ++k) {
      const unsigned sp = bfbits(silu_f(lo16(cp2[k]))) | (bfbits(silu_f(hi16(cp2[k]))) << 16);
#pragma unroll
      for (int i = 0; i < 4; ++i) { a8[2 * i] = dot2(plo(w[2 * k + 1][i], w[2 * k][i]), sp, a8[2 * i]); a8[2 * i + 1] = dot2(phi(w[2 * k + 1][i], w[2 * k][i]), sp, a8[2 * i + 1]); }
    }
    if (lane < 50) {
      *reinterpret_cast<float4*>(wred + mrg * 40 + mcc * 8) = float4{a8[0], a8[1], a8[2], a8[3]};
      *reinterpret_cast<float4*>(wred + mrg * 40 + mcc * 8 + 4) = float4{a8[4], a8[5], a8[6], a8[7]};
    }
    lds_wait();
    unsigned old = 0;
    if (lane == 0) old = __hip_atomic_fetch_add(lds + L_CNT + 1, 1u, __ATOMIC_RELAXED, __HIP_MEMORY_SCOPE_WORKGROUP);
    old = __builtin_amdgcn_readfirstlane(old);
    asm volatile("" ::: "memory");
    if (old == 3u) {
      float p4[4] = {0.f, 0.f, 0.f, 0.f};
#pragma unroll
      for (int g = 0; g < 10; ++g)
#pragma unroll
        for (int w4 = 0; w4 < 4; ++w4) p4[w4] += ldf[L_RED + w4 * 480 + g * 40 + gl];
      if (lane < 40) st8(rws, WS_M2 + (40 * m2 + lane) * 8, u32x2{bfbits((p4[0] + p4[1]) + (p4[2] + p4[3])) | tg.tl, tg.th});
      lds_wait();
      if (lane == 0) __hip_atomic_store(lds + L_CNT + 1, 8u, __ATOMIC_RELAXED, __HIP_MEMORY_SCOPE_WORKGROUP);
    }
    for (unsigned spin = 0; spin < (SPIN_LIMIT << 4); ++spin) {
      if (__hip_atomic_load(lds + L_CNT + 1, __ATOMIC_RELAXED, __HIP_MEMORY_SCOPE_WORKGROUP) >= 8u) break;
      asm volatile("s_sleep 2" ::: "memory");
    }
    asm volatile("" ::: "memory");
  }
  const int org = lane / 10, ocg = lane - 10 * org;
  unsigned wop[4][8];
  {
    const rsrc_t rwo = mk(wo, HD * D * 2);
    const unsigned o0 = lane < 60 ? ((h * DH + 48 * vw + 8 * org) * D + 80 * j + 8 * ocg) * 2 : OOB;
    u32x4 w[8];
#pragma unroll
    for (int k = 0; k < 8; ++k) w[k] = __builtin_amdgcn_raw_buffer_load_b128(rwo, o0, k * D * 2, AUX_NT);
#pragma unroll
    for (int p = 0; p < 4; ++p)
#pragma unroll
      for (int i = 0; i < 4; ++i) { wop[p][2 * i] = plo(w[2 * p + 1][i], w[2 * p][i]); wop[p][2 * i + 1] = phi(w[2 * p + 1][i], w[2 * p][i]); }
  }
  auto h1_stage = [&]() {
    bar();
    if (vw == 0) STAMP(4);
    {
      const float m0 = ldf[L_PART + 192], m1 = ldf[L_PART + 200 + 192], m2 = ldf[L_PART + 400 + 192], m3 = ldf[L_PART + 600 + 192];
      const float M = fmaxf(fmaxf(m0, m1), fmaxf(m2, m3));
      const float e0 = __expf(m0 - M), e1 = __expf(m1 - M), e2 = __expf(m2 - M), e3 = __expf(m3 - M);
      const int idx = vt < 192 ? vt : 193;
      float v = e0 * ldf[L_PART + idx];
      v = fmaf(e1, ldf[L_PART + 200 + idx], v); v = fmaf(e2, ldf[L_PART + 400 + idx], v); v = fmaf(e3, ldf[L_PART + 600 + idx], v);
      if (vt == 192) v = M;
      if (vt < 194) st16(rws, WS_R + (b * 200 + vt) * 16, enc2(v, tg));
    }
    {
      const int s = lane >> 3, dg = lane & 7;
      u32x4 g[8];
      const unsigned rb = WS_R + (h * 8 + s) * 200 * 16, r0 = rb + (48 * vw + dg) * 16;
      poll<8>([&](int k) { return __builtin_amdgcn_raw_buffer_load_b128(rws, k < 6 ? r0 + k * 128 : rb + (186 + k) * 16, 0, AUX_SC1); }, tg, g);
      if (vw == 0) STAMP(15);
      const float ms = dec2(g[6]), M = max_hi3(ms), e = __expf(ms - M);
      const float L = sum_hi3(e * dec2(g[7])), rl = __builtin_amdgcn_rcpf(L);
      float z[3];
#pragma unroll
      for (int k = 0; k < 3; ++k) z[k] = tr32(e * dec2(g[k]), e * dec2(g[k + 3]));
      const float z01 = tr16(z[0], z[1]), z2 = tr16(z[2], z[2]);
      const float ov = tr8(z01, z2, (lane & 8) != 0);
      u16* op = reinterpret_cast<u16*>(lds + L_OP + vw * 24) + dg + 24 * (lane >> 5);
      if ((lane & 24) != 24) op[(lane & 8) ? 16 : 8 * ((lane >> 4) & 1)] = (u16)bfbits(fdiv(ov, L, rl));
      lds_wait();
      const u32x4 opv = *reinterpret_cast<const u32x4*>(lds + L_OP + vw * 24 + 4 * (org < 6 ? org : 0));
      float y[8];
#pragma unroll
      for (int i = 0; i < 8; ++i) y[i] = 0.f;
#pragma unroll
      for (int p = 0; p < 4; ++p)
#pragma unroll
        for (int i = 0; i < 8; ++i) y[i] = dot2(wop[p][i], opv[p], y[i]);
      if (lane < 60) {
        float* dst = ldf + L_RED + (vw * 6 + org) * 80 + 8 * ocg;
        *reinterpret_cast<float4*>(dst) = float4{y[0], y[1], y[2], y[3]};
        *reinterpret_cast<float4*>(dst + 4) = float4{y[4], y[5], y[6], y[7]};
      }
    }
    bar();
    if (vw < 3) {
      const int yc = vt < 160 ? vt >> 1 : 79;
      const float* src = ldf + L_RED + (vt & 1) * 960 + yc;
      float p4[4] = {0.f, 0.f, 0.f, 0.f};
#pragma unroll
      for (int g = 0; g < 12; ++g) p4[g & 3] += src[g * 80];
      float y = (p4[0] + p4[1]) + (p4[2] + p4[3]);
      y += dpp<XOR1>(y);
      if (vt < 160 && (vt & 1) == 0) st16(rws, WS_Y + (((20 * j + (yc >> 2)) * 32 + h) * 4 + (yc & 3)) * 16, enc2(y, tg));
    }
    if (vw == 0) STAMP(5);
  };

  if (j == 2 || j == 3) {
    const int f = 2 * h + j - 2, rg = vt >> 3, cc = vt & 7;
    if (vw < 3) {
      const rsrc_t rwd = mk(wd, F * D * 2);
      const unsigned o0 = vt < 160 ? (24 * f * D + 4 * vt) * 2 : OOB;
      u32x2 w[24];
#pragma unroll
      for (int k = 0; k < 24; ++k) w[k] = __builtin_amdgcn_raw_buffer_load_b64(rwd, o0, k * D * 2, AUX_NT);
      if (vt < 160) {
#pragma unroll
        for (int p = 0; p < 12; ++p)
          *reinterpret_cast<u32x4*>(lds + L_WD + (p * 160 + vt) * 4) =
              u32x4{plo(w[2 * p + 1][0], w[2 * p][0]), phi(w[2 * p + 1][0], w[2 * p][0]), plo(w[2 * p + 1][1], w[2 * p][1]), phi(w[2 * p + 1][1], w[2 * p][1])};
      }
    }
    unsigned gp[10][8];
    {
      const rsrc_t rg_ = mk(wg, D * F * 2), ru_ = mk(wu, D * F * 2);
      const unsigned og = cc < 3 ? (20 * rg * F + 24 * f + 8 * cc) * 2 : OOB, ou = (cc >= 3 && cc < 6) ? (20 * rg * F + 24 * f + 8 * (cc - 3)) * 2 : OOB;
#pragma unroll
      for (int hf = 0; hf < 2; ++hf) {
        u32x4 w[10], w2[10];
#pragma unroll
        for (int k = 0; k < 10; ++k) {
          w[k] = __builtin_amdgcn_raw_buffer_load_b128(rg_, og, (10 * hf + k) * F * 2, AUX_NT);
          w2[k] = __builtin_amdgcn_raw_buffer_load_b128(ru_, ou, (10 * hf + k) * F * 2, AUX_NT);
        }
#pragma unroll
        for (int k = 0; k < 10; ++k) w[k] |= w2[k];
#pragma unroll
        for (int p = 0; p < 5; ++p)
#pragma unroll
          for (int i = 0; i < 4; ++i) { gp[5 * hf + p][2 * i] = plo(w[2 * p + 1][i], w[2 * p][i]); gp[5 * hf + p][2 * i + 1] = phi(w[2 * p + 1][i], w[2 * p][i]); }
      }
#pragma unroll
      for (int p = 0; p < 10; ++p)
#pragma unroll
        for (int i = 0; i < 8; ++i) asm volatile("" : "+v"(gp[p][i]));
    }
    unsigned po[5];
#pragma unroll
    for (int k = 0; k < 5; ++k) { const int p = 80 * vw + lane + 64 * k; po[k] = (unsigned)(p >= 320 ? p - 320 : p) * 16u; }
    float sbv[4], c1v[4];
    {
      u32x4 g[4];
      const unsigned off[4] = {WS_M2 + D * 8 + po[0], WS_M2 + D * 8 + po[1], WS_M2 + 2 * D * 8 + po[0], WS_M2 + 2 * D * 8 + po[1]};
      gather_slow<4>(rws, off, tg, g);
#pragma unroll
      for (int k = 0; k < 2; ++k) {
        const unsigned sp = decp(g[k]), cq = decp(g[2 + k]);
        sbv[2 * k] = lo16(sp); sbv[2 * k + 1] = hi16(sp); c1v[2 * k] = rbf(1.f + lo16(cq)); c1v[2 * k + 1] = rbf(1.f + hi16(cq));
      }
    }
    h1_stage();
    u32x4 wdr[12];
#pragma unroll
    for (int p = 0; p < 12; ++p) wdr[p] = *reinterpret_cast<const u32x4*>(lds + L_WD + (p * 160 + (vt < 160 ? vt : 0)) * 4);
    {
      u32x4 g[5];
      poll<5>([&](int k) { return __builtin_amdgcn_raw_buffer_load_b128(rws, WS_X1 + po[k], 0, AUX_SC1); }, tg, g);
      if (vw == 0) STAMP(6);
      float xv[10];
#pragma unroll
      for (int k = 0; k < 5; ++k) { xv[2 * k] = __uint_as_float(g[k][0] << 16); xv[2 * k + 1] = __uint_as_float(g[k][2] << 16); }
      const float s = ((xv[0] + xv[1]) + (xv[2] + xv[3])) + ((xv[4] + xv[5]) + (xv[6] + xv[7])) + (xv[8] + xv[9]);
      const float mean = sum_wave(s) * (1.f / D);
      float v0 = 0.f, v1 = 0.f;
#pragma unroll
      for (int k = 0; k < 5; ++k) { const float d0 = xv[2 * k] - mean, d1 = xv[2 * k + 1] - mean; v0 = fmaf(d0, d0, v0); v1 = fmaf(d1, d1, v1); }
      const float var = sum_wave(v0 + v1) * (1.f / D);
      const float mb2 = rbf(mean), den2 = rbf(sqrtf(rbf(rbf(var) + 1e-5f))), rd = __builtin_amdgcn_rcpf(den2);
#pragma unroll
      for (int k = 0; k < 2; ++k) {
        const float n0 = rbf(fdiv(rbf(xv[2 * k] - mb2), den2, rd)), n1 = rbf(fdiv(rbf(xv[2 * k + 1] - mb2), den2, rd));
        const float h0 = rbf(n0 * c1v[2 * k]) + sbv[2 * k], h1 = rbf(n1 * c1v[2 * k + 1]) + sbv[2 * k + 1];
        if (k == 0 || lane < 16) lds[L_H2 + vw * 80 + lane + 64 * k] = bfbits(h0) | (bfbits(h1) << 16);
      }
    }
    lds_wait();
    {
      float a8[8];
#pragma unroll
      for (int i = 0; i < 8; ++i) a8[i] = 0.f;
#pragma unroll
      for (int p2 = 0; p2 < 5; ++p2) {
        const u32x2 hp = *reinterpret_cast<const u32x2*>(lds + L_H2 + vw * 80 + 10 * (lane >> 3) + 2 * p2);
#pragma unroll
        for (int i = 0; i < 8; ++i) { a8[i] = dot2(gp[2 * p2][i], hp[0], a8[i]); a8[i] = dot2(gp[2 * p2 + 1][i], hp[1], a8[i]); }
      }
      float z1[4], z2[2];
#pragma unroll
      for (int k = 0; k < 4; ++k) z1[k] = tr32(a8[k], a8[k + 4]);
#pragma unroll
      for (int k = 0; k < 2; ++k) z2[k] = tr16(z1[k], z1[k + 2]);
      const float z3 = tr8(z2[0], z2[1], (lane & 8) != 0);
      ldf[L_GU + vw * 64 + cc * 8 + 4 * (lane >> 5) + 2 * ((lane >> 4) & 1) + ((lane >> 3) & 1)] = z3;
    }
    bar();
    if (vw < 3) {
      const int n = lane < 24 ? lane : 0;
      const float g = (ldf[L_GU + n] + ldf[L_GU + 64 + n]) + (ldf[L_GU + 128 + n] + ldf[L_GU + 192 + n]);
      const float u = (ldf[L_GU + 24 + n] + ldf[L_GU + 88 + n]) + (ldf[L_GU + 152 + n] + ldf[L_GU + 216 + n]);
      const unsigned ab = bfbits(rbf(silu_f(rbf(g))) * rbf(u));
      const unsigned nb = (unsigned)__builtin_amdgcn_mov_dpp((int)ab, XOR1, 0xf, 0xf, true);
      const unsigned ap = ab | (nb << 16);
      float y[4] = {0.f, 0.f, 0.f, 0.f};
#pragma unroll
      for (int p = 0; p < 12; ++p) {
        const unsigned a2 = (unsigned)__builtin_amdgcn_readlane((int)ap, 2 * p);
#pragma unroll
        for (int i = 0; i < 4; ++i) y[i] = dot2(wdr[p][i], a2, y[i]);
      }
      const bool o1 = (lane & 1) != 0, o2 = (lane & 2) != 0;
      { const float s0 = o1 ? y[0] : y[1], s1 = o1 ? y[2] : y[3]; const float r0 = dpp<XOR1>(s0), r1 = dpp<XOR1>(s1);
        if (o1) { y[0] = r0; y[2] = r1; } else { y[1] = r0; y[3] = r1; } }
      { const float s0 = o2 ? y[0] : y[2], s1 = o2 ? y[1] : y[3]; const float r0 = dpp<XOR2>(s0), r1 = dpp<XOR2>(s1);
        if (o2) { y[0] = r0; y[1] = r1; } else { y[2] = r0; y[3] = r1; } }
      if (vt < 160) {
#pragma unroll
        for (int i = 0; i < 4; ++i) st16(rws, WS_O + ((((vt & ~3) + i) * NFFN + f) * 4 + (vt & 3)) * 16, enc2(y[i], tg));
      }
    }
    if (vw == 0) STAMP(10);
  } else {
    const bool red = j != 4 && vw == 3;
    const int rr = 5 * h + (j < 2 ? j : j - 3), col = 4 * rr + (lane & 3);
    float ga = 0.f, gb = 0.f, xv = 0.f;
    if (red) {
      u32x4 g[2];
      const unsigned off[2] = {WS_M2 + (col & ~1) * 8, WS_M2 + (col & ~1) * 8 + 3 * D * 8};
      gather_slow<2>(rws, off, tg, g);
      const unsigned pa = decp(g[0]), pb = decp(g[1]);
      ga = (col & 1) ? hi16(pa) : lo16(pa); gb = (col & 1) ? hi16(pb) : lo16(pb);
      xv = __uint_as_float((unsigned)x[col] << 16);
    }
    h1_stage();
    if (red) {
      float x1;
      {
        u32x4 g[2];
        gather<2>(rws, WS_Y + (rr * 128 + lane) * 16, 64 * 16, tg, g);
        float y = dec2(g[0]) + dec2(g[1]);
        y += dpp<ROR4>(y); y = sum_hi3(y);
        x1 = rbf(xv + rbf(ga * rbf(y)));
        if (lane < 4) st8(rws, WS_X1 + col * 8, u32x2{bfbits(x1) | tg.tl, tg.th});
      }
      STAMP(3);
      {
        u32x4 g[4];
        gather<4>(rws, WS_O + (rr * NFFN * 4 + lane) * 16, 64 * 16, tg, g);
        float f = (dec2(g[0]) + dec2(g[1])) + (dec2(g[2]) + dec2(g[3]));
        f += dpp<ROR4>(f); f = sum_hi3(f);
        const float o = x1 + rbf(gb * rbf(f));
        if (lane < 4) out[col] = (u16)bfbits(o);
        if (b == 0 && lane == 0) *reinterpret_cast<u64*>(ws + WS_EPOCH) = E1;
      }
      STAMP(7);
    }
  }
}

extern "C" void kernel_launch(void* const* d_in, const int* in_sizes, int n_in, void* d_out, int out_size, void* d_ws, size_t ws_size, hipStream_t stream) {
  (void)in_sizes; (void)n_in; (void)out_size; (void)ws_size;
  k_block<<<NWG, 512, 0, stream>>>((const u16*)d_in[0], (const u16*)d_in[1], (const u16*)d_in[2], (const u16*)d_in[3], (const u16*)d_in[4],
                                   (const u16*)d_in[6], (const u16*)d_in[7], (const u16*)d_in[8], (const u16*)d_in[9], (const u16*)d_in[10],
                                   (u16*)d_out, (unsigned char*)d_ws);
}
```

```cpp
#include <hip/hip_runtime.h>
#include <cstdint>

typedef unsigned short u16;
typedef unsigned long long u64;
using u32x4 = __attribute__((ext_vector_type(4))) unsigned;
using u32x2 = __attribute__((ext_vector_type(2))) unsigned;
using bf16x2 = __attribute__((ext_vector_type(2))) __bf16;

#define KU 2
#define SPCT 75
#define PF 12
#define NL 2
#define PSLEEP 10
#define ROT 0
#define DLY 0
#define ZEND 192
#define STAMPS 0

constexpr int D = 640, H = 32, SK = 32768, DH = 192, F = 1536, HD = H * DH;
constexpr int NWG = 256, NFFN = 64;
constexpr unsigned WS_EPOCH = 0;
constexpr unsigned WS_CTR = 1024;
constexpr unsigned WS_P1 = 16384;
constexpr unsigned WS_P2 = WS_P1 + 2 * 1280 * 16;
constexpr unsigned WS_M2 = WS_P2 + 32 * 2 * 192 * 16;
constexpr unsigned WS_R = WS_M2 + 2560 * 8;
constexpr unsigned WS_Y = WS_R + 256 * 200 * 16;
constexpr unsigned WS_X1 = WS_Y + 160 * 32 * 64;
constexpr unsigned WS_O = WS_X1 + 640 * 8;
constexpr unsigned WS_ST = WS_O + 160 * NFFN * 64;
constexpr unsigned WS_END = WS_ST + 256 * 16 * 8;

__device__ __forceinline__ float rbf(float x) { return (float)(__bf16)x; }
__device__ __forceinline__ unsigned bfbits(float x) { return (unsigned)__builtin_bit_cast(u16, (__bf16)x); }
__device__ __forceinline__ float lo16(unsigned v) { return __uint_as_float(v << 16); }
__device__ __forceinline__ float hi16(unsigned v) { return __uint_as_float(v & 0xffff0000u); }
__device__ __forceinline__ float dot2(unsigned a, unsigned b, float acc) {
  return __builtin_amdgcn_fdot2_f32_bf16(__builtin_bit_cast(bf16x2, a), __builtin_bit_cast(bf16x2, b), acc, false);
}
template <int CTRL> __device__ __forceinline__ float dpp(float x) {
  return __builtin_bit_cast(float, __builtin_amdgcn_mov_dpp(__builtin_bit_cast(int, x), CTRL, 0xf, 0xf, true));
}
constexpr int XOR1 = 0xB1, XOR2 = 0x4E, HMIR = 0x141, ROR8 = 0x128, ROR4 = 0x124;
__device__ __forceinline__ float tr32(float a, float b) {
  auto s = __builtin_amdgcn_permlane32_swap(__float_as_uint(a), __float_as_uint(b), false, false);
  return __uint_as_float(s[0]) + __uint_as_float(s[1]);
}
__device__ __forceinline__ float tr16(float a, float b) {
  auto s = __builtin_amdgcn_permlane16_swap(__float_as_uint(a), __float_as_uint(b), false, false);
  return __uint_as_float(s[0]) + __uint_as_float(s[1]);
}
__device__ __forceinline__ float tr8(float a, float b, bool bit3) { const float t1 = a + dpp<ROR8>(a), t2 = b + dpp<ROR8>(b); return bit3 ? t2 : t1; }
__device__ __forceinline__ float mx32(float a) { auto s = __builtin_amdgcn_permlane32_swap(__float_as_uint(a), __float_as_uint(a), false, false); return fmaxf(__uint_as_float(s[0]), __uint_as_float(s[1])); }
__device__ __forceinline__ float mx16(float a) { auto s = __builtin_amdgcn_permlane16_swap(__float_as_uint(a), __float_as_uint(a), false, false); return fmaxf(__uint_as_float(s[0]), __uint_as_float(s[1])); }
__device__ __forceinline__ float sum_hi3(float a) { a += dpp<ROR8>(a); a = tr16(a, a); return tr32(a, a); }
__device__ __forceinline__ float max_hi3(float a) { a = fmaxf(a, dpp<ROR8>(a)); return mx32(mx16(a)); }
__device__ __forceinline__ float sum_row16(float a) { a += dpp<XOR1>(a); a += dpp<XOR2>(a); a += dpp<HMIR>(a); a += dpp<ROR8>(a); return a; }
__device__ __forceinline__ float sum_wave(float a) { a = sum_row16(a); a = tr16(a, a); return tr32(a, a); }
__device__ __forceinline__ unsigned plo(unsigned r1, unsigned r0) { return __builtin_amdgcn_perm(r1, r0, 0x05040100u); }
__device__ __forceinline__ unsigned phi(unsigned r1, unsigned r0) { return __builtin_amdgcn_perm(r1, r0, 0x07060302u); }
__device__ __forceinline__ float silu_f(float x) { return x / (1.f + __expf(-x)); }
__device__ __forceinline__ void bar() { asm volatile("s_waitcnt lgkmcnt(0)" ::: "memory"); __builtin_amdgcn_s_barrier(); asm volatile("" ::: "memory"); }

typedef __amdgpu_buffer_rsrc_t rsrc_t;
__device__ __forceinline__ rsrc_t mk(const void* p, unsigned bytes) { return __builtin_amdgcn_make_buffer_rsrc((void*)p, 0, bytes, 0x00020000); }
constexpr int AUX_NT = 2, AUX_SC1 = 16;

struct Tag { unsigned tl, th; };
__device__ __forceinline__ u32x4 enc2(float v, Tag t) { const unsigned b = __float_as_uint(v); return u32x4{(b & 0xffffu) | t.tl, t.th, (b >> 16) | t.tl, t.th}; }
__device__ __forceinline__ float dec2(u32x4 g) { return __uint_as_float((g[0] & 0xffffu) | (g[2] << 16)); }
__device__ __forceinline__ unsigned decp(u32x4 g) { return (g[0] & 0xffffu) | (g[2] << 16); }
__device__ __forceinline__ void st16(rsrc_t r, unsigned off, u32x4 v) { __builtin_amdgcn_raw_buffer_store_b128(v, r, off, 0, AUX_SC1); }
__device__ __forceinline__ void st8(rsrc_t r, unsigned off, u32x2 v) { __builtin_amdgcn_raw_buffer_store_b64(v, r, off, 0, AUX_SC1); }

constexpr unsigned SPIN_LIMIT = 1u << 19;
constexpr unsigned OOB = 0x80000000u;
__device__ __forceinline__ unsigned bad2(u32x4 g, Tag t) { return (((g[0] ^ t.tl) | (g[2] ^ t.tl)) >> 16) | (g[1] ^ t.th) | (g[3] ^ t.th); }
template <int N, class LD>
__device__ __forceinline__ void poll(LD ld, Tag t, u32x4 (&g)[N]) {
  u32x4 y[N];
#pragma unroll
  for (int k = 0; k < N; ++k) g[k] = ld(k);
  asm volatile("s_sleep %0" :: "i"(PSLEEP) : "memory");
  for (unsigned spin = 0; spin < SPIN_LIMIT; ++spin) {
#pragma unroll
    for (int k = 0; k < N; ++k) y[k] = ld(k);
    unsigned bad = 0;
#pragma unroll
    for (int k = 0; k < N; ++k) bad |= bad2(g[k], t);
    if (__builtin_amdgcn_ballot_w64(bad != 0u) == 0ull) break;
    asm volatile("" ::: "memory");
#pragma unroll
    for (int k = 0; k < N; ++k) g[k] = ld(k);
    bad = 0;
#pragma unroll
    for (int k = 0; k < N; ++k) bad |= bad2(y[k], t);
    if (__builtin_amdgcn_ballot_w64(bad != 0u) == 0ull) {
#pragma unroll
      for (int k = 0; k < N; ++k) g[k] = y[k];
      break;
    }
    asm volatile("" ::: "memory");
  }
}
template <int N>
__device__ __forceinline__ void gather(rsrc_t r, unsigned off, unsigned stride, Tag t, u32x4 (&g)[N]) {
  poll<N>([&](int k) { return __builtin_amdgcn_raw_buffer_load_b128(r, off + k * stride, 0, AUX_SC1); }, t, g);
}
template <int N>
__device__ __forceinline__ void gather_slow(rsrc_t r, const unsigned (&off)[N], Tag t, u32x4 (&g)[N]) {
  for (unsigned spin = 0; spin < SPIN_LIMIT; ++spin) {
#pragma unroll
    for (int k = 0; k < N; ++k) g[k] = __builtin_amdgcn_raw_buffer_load_b128(r, off[k], 0, AUX_SC1);
    unsigned bad = 0;
#pragma unroll
    for (int k = 0; k < N; ++k) bad |= bad2(g[k], t);
    if (__builtin_amdgcn_ballot_w64(bad != 0u) == 0ull) break;
    asm volatile("s_sleep 16" ::: "memory");
  }
}
__device__ __forceinline__ void lds_wait() { asm volatile("s_waitcnt lgkmcnt(0)" ::: "memory"); }
__device__ __forceinline__ float fdiv(float a, float d, float r) { const float q = a * r; return fmaf(fmaf(-q, d, a), r, q); }

#define STAMP(i) do { } while (0)

constexpr int L_RED = 0;
constexpr int L_HS = L_RED + 1920;
constexpr int L_Q = L_HS + 160;
constexpr int L_PART = L_Q + 96;
constexpr int L_OP = L_PART + 800;
constexpr int L_H2 = L_OP + 96;
constexpr int L_GU = L_H2 + 320;
constexpr int L_CNT = L_GU + 256;
constexpr int L_WD = L_CNT + 4;
constexpr int L_KV = L_WD + 7680;
constexpr int KVW = (NL > 0 ? NL : 1) * KU * 1536;
constexpr int L_TOTAL = L_KV + 4 * KVW < 24576 ? 24576 : L_KV + 4 * KVW;
static_assert(L_WD % 4 == 0 && L_KV % 4 == 0 && L_TOTAL * 4 <= 163840, "lds map");

__global__ void __launch_bounds__(512, 2)
k_block(const u16* __restrict__ x, const u16* __restrict__ Kc, const u16* __restrict__ Vc, const u16* __restrict__ wq, const u16* __restrict__ wo,
        const u16* __restrict__ wg, const u16* __restrict__ wu, const u16* __restrict__ wd, const u16* __restrict__ cnd, const u16* __restrict__ wmod,
        u16* __restrict__ out, unsigned char* __restrict__ ws) {
  __shared__ __attribute__((aligned(16))) unsigned lds[L_TOTAL];
  float* ldf = reinterpret_cast<float*>(lds);
  const int tid = threadIdx.x, lane = tid & 63, wv = __builtin_amdgcn_readfirstlane(tid >> 6);
  const int b = blockIdx.x, h = b >> 3, j = b & 7;
  const rsrc_t rws = mk(ws, WS_END);
  const rsrc_t rmod = mk(wmod, D * 6 * D * 2), rc = mk(cnd, D * 2);
  const int mrg = lane / 5, mcc = lane - 5 * mrg;
  const int gl = lane < 40 ? lane : 39;
  if (wv == 4) STAMP(0);
  const u32x2 ev = __builtin_amdgcn_raw_buffer_load_b64(rws, WS_EPOCH, 0, AUX_SC1);
  if (tid == 511) { lds[L_CNT] = 0u; lds[L_CNT + 1] = 0u; }

  if (j == 2 || j == 3) {
    const int kb = j - 2;
    u32x4 wm[4]; unsigned cpair[2];
    const unsigned ob = lane < 50 ? 0u : OOB;
#pragma unroll
    for (int k = 0; k < 4; ++k) wm[k] = __builtin_amdgcn_raw_buffer_load_b128(rmod, ((320 * kb + 40 * wv + 2 * (mrg + 10 * (k >> 1)) + (k & 1)) * (6 * D) + 40 * h + 8 * mcc) * 2 | ob, 0, AUX_NT);
#pragma unroll
    for (int k = 0; k < 2; ++k) cpair[k] = __builtin_amdgcn_raw_buffer_load_b32(rc, (320 * kb + 40 * wv + 2 * (mrg + 10 * k)) * 2 | ob, 0, 0);
    float a8[8];
#pragma unroll
    for (int i = 0; i < 8; ++i) a8[i] = 0.f;
#pragma unroll
    for (int k = 0; k < 2; ++k) {
      const unsigned sp = bfbits(silu_f(lo16(cpair[k]))) | (bfbits(silu_f(hi16(cpair[k]))) << 16);
#pragma unroll
      for (int i = 0; i < 4; ++i) { a8[2 * i] = dot2(plo(wm[2 * k + 1][i], wm[2 * k][i]), sp, a8[2 * i]); a8[2 * i + 1] = dot2(phi(wm[2 * k + 1][i], wm[2 * k][i]), sp, a8[2 * i + 1]); }
    }
    if (lane < 50) {
      float* d_ = ldf + L_WD + (wv * 10 + mrg) * 40 + mcc * 8;
      *reinterpret_cast<float4*>(d_) = float4{a8[0], a8[1], a8[2], a8[3]};
      *reinterpret_cast<float4*>(d_ + 4) = float4{a8[4], a8[5], a8[6], a8[7]};
    }
    const u64 E1 = (((u64)ev[1] << 32) | ev[0]) + 1ull;
    Tag tg; tg.tl = (unsigned)(E1 & 0xffffu) << 16; tg.th = (unsigned)(E1 >> 16);
    bar();
    if (wv >= 3) {
      const int t = tid - 192;
      const float* src = ldf + L_WD + (t & 7) * 400 + (t >> 3);
      float s0 = 0.f, s1 = 0.f;
#pragma unroll
      for (int g = 0; g < 5; ++g) { s0 += src[g * 40]; s1 += src[(g + 5) * 40]; }
      float s = s0 + s1;
      s += dpp<XOR1>(s); s += dpp<XOR2>(s); s += dpp<HMIR>(s);
      if ((t & 7) == 0) st16(rws, WS_P1 + (kb * 1280 + 40 * h + (t >> 3)) * 16, enc2(s, tg));
      if (wv == 4) STAMP(11);
    }
    bar();
  } else if (j < 2) {
    unsigned xp[5];
    if (j == 0 && tid == 0) (void)__hip_atomic_exchange((unsigned*)(ws + WS_CTR + h * 256), 0u, __ATOMIC_RELAXED, __HIP_MEMORY_SCOPE_AGENT);
#pragma unroll
    for (int k = 0; k < 5; ++k) xp[k] = *reinterpret_cast<const unsigned*>(x + 2 * (lane + 64 * k));
    const int hr = tid < 320 ? tid : 319;
    const float xi = __uint_as_float((unsigned)x[320 * j + hr] << 16);
    asm volatile("" ::: "memory");
    const int qr = lane / 12, qc = lane - 12 * qr;
    u32x4 wqr[16];
    {
      const rsrc_t rwq = mk(wq, D * HD * 2);
      const unsigned o0 = lane < 60 ? ((320 * j + 80 * (wv >> 1) + 2 * qr) * HD + h * DH + 96 * (wv & 1) + 8 * qc) * 2 : OOB;
#pragma unroll
      for (int k = 0; k < 16; ++k) wqr[k] = __builtin_amdgcn_raw_buffer_load_b128(rwq, o0, (10 * (k >> 1) + (k & 1)) * HD * 2, AUX_NT);
    }
    asm volatile("" ::: "memory");
    const u64 E1 = (((u64)ev[1] << 32) | ev[0]) + 1ull;
    Tag tg; tg.tl = (unsigned)(E1 & 0xffffu) << 16; tg.th = (unsigned)(E1 >> 16);
    if (wv < 5) {
      float sx = 0.f;
#pragma unroll
      for (int k = 0; k < 5; ++k) sx += lo16(xp[k]) + hi16(xp[k]);
      const float mean = sum_wave(sx) * (1.f / D);
      float v = 0.f;
#pragma unroll
      for (int k = 0; k < 5; ++k) { const float d0 = lo16(xp[k]) - mean, d1 = hi16(xp[k]) - mean; v = fmaf(d0, d0, v); v = fmaf(d1, d1, v); }
      const float var = sum_wave(v) * (1.f / D);
      const float mb = rbf(mean), den = rbf(sqrtf(rbf(rbf(var) + 1e-5f)));
      float nrm = rbf(rbf(xi - mb) / den);
      asm volatile("" : "+v"(nrm));
      u32x4 g[4];
      const unsigned p0 = WS_P1 + (320 * j + tid) * 16;
      poll<4>([&](int k) { return __builtin_amdgcn_raw_buffer_load_b128(rws, p0 + (k & 1) * 1280 * 16 + (k >> 1) * 640 * 16, 0, AUX_SC1); }, tg, g);
      const float sa = dec2(g[0]) + dec2(g[1]), ca = dec2(g[2]) + dec2(g[3]);
      const float hv = rbf(nrm * rbf(1.f + rbf(ca))) + rbf(sa);
      reinterpret_cast<u16*>(lds + L_HS)[tid] = (u16)bfbits(hv);
      if (wv == 0) STAMP(12);
    }
    unsigned wqp[8][8];
#pragma unroll
    for (int k = 0; k < 8; ++k)
#pragma unroll
      for (int i = 0; i < 4; ++i) { wqp[k][2 * i] = plo(wqr[2 * k + 1][i], wqr[2 * k][i]); wqp[k][2 * i + 1] = phi(wqr[2 * k + 1][i], wqr[2 * k][i]); }
    asm volatile("s_waitcnt vmcnt(0)" ::: "memory");
    bar();
    {
      float a8[8];
#pragma unroll
      for (int i = 0; i < 8; ++i) a8[i] = 0.f;
#pragma unroll
      for (int k = 0; k < 8; ++k) {
        const unsigned hp = lds[L_HS + 40 * (wv >> 1) + (qr < 5 ? qr : 0) + 5 * k];
#pragma unroll
        for (int i = 0; i < 8; ++i) a8[i] = dot2(wqp[k][i], hp, a8[i]);
      }
      if (lane < 60) {
        float* d_ = ldf + L_WD + 3200 + ((wv >> 1) * 5 + qr) * 192 + 96 * (wv & 1) + qc * 8;
        *reinterpret_cast<float4*>(d_) = float4{a8[0], a8[1], a8[2], a8[3]};
        *reinterpret_cast<float4*>(d_ + 4) = float4{a8[4], a8[5], a8[6], a8[7]};
      }
    }
    bar();
    if (wv >= 2) {
      const int t = tid - 128;
      const float* src = ldf + L_WD + 3200 + (t & 1) * 1920 + (t >> 1);
      float s0 = 0.f, s1 = 0.f;
#pragma unroll
      for (int g = 0; g < 5; ++g) { s0 += src[g * 192]; s1 += src[(g + 5) * 192]; }
      float s = s0 + s1;
      s += dpp<XOR1>(s);
      if ((t & 1) == 0) st16(rws, WS_P2 + ((h * 2 + j) * 192 + (t >> 1)) * 16, enc2(s, tg));
      if (wv == 4) STAMP(13);
    }
    bar();
  }

  if (wv < 4) {
    constexpr int U = KU;
    constexpr int NW = 32;
    constexpr int TPG = SK / (8 * U);
    constexpr int S = (TPG / NW) * SPCT / 100;
    static_assert(S >= NL + 8, "queue");
    const int r = lane >> 3, c = lane & 7, rnk = j * 4 + wv;
    const unsigned loff = r * 384 + c * 16;
    const rsrc_t rk = mk((const char*)Kc + (size_t)h * SK * 384, SK * 384), rv = mk((const char*)Vc + (size_t)h * SK * 384, SK * 384);
    const unsigned* cp = (const unsigned*)(ws + WS_CTR + h * 256);
    u32x4 A[2 * U][3], B[2 * U][3], C[2 * U][3];
#define TOFF(tile) ((unsigned)(ROT ? ((tile) + h * ROT) & (TPG - 1) : (tile)) * (3072u * U))
#define LOADT(X, tile) do { const unsigned so_ = TOFF(tile), vo_ = (tile) < TPG ? loff : OOB; \
    _Pragma("unroll") for (int u = 0; u < 2 * U; ++u) _Pragma("unroll") for (int q_ = 0; q_ < 3; ++q_) \
      X[u][q_] = __builtin_amdgcn_raw_buffer_load_b128(u < U ? rk : rv, vo_ + (u % U) * 3072 + q_ * 128, so_, AUX_NT); } while (0)
#define LOADP(X, tile, i0) do { const unsigned so_ = TOFF(tile); \
    _Pragma("unroll") for (int u = 0; u < 2 * U; ++u) _Pragma("unroll") for (int q_ = 0; q_ < 3; ++q_) { \
      if ((i0) + u * 3 + q_ >= PF) asm volatile("s_waitcnt vmcnt(%0)" :: "i"(PF > 63 ? 63 : PF - 1) : "memory"); \
      X[u][q_] = __builtin_amdgcn_raw_buffer_load_b128(u < U ? rk : rv, loff + (u % U) * 3072 + q_ * 128, so_, AUX_NT); } } while (0)
#define USE(X) do { float s_[U]; float mn_ = m; \
    _Pragma("unroll") for (int u = 0; u < U; ++u) { float d_ = 0.f; \
      _Pragma("unroll") for (int q_ = 0; q_ < 3; ++q_) _Pragma("unroll") for (int i = 0; i < 4; ++i) d_ = dot2(X[u][q_][i], qv[q_][i], d_); \
      d_ += dpp<XOR1>(d_); d_ += dpp<XOR2>(d_); d_ += dpp<HMIR>(d_); \
      s_[u] = rbf(rbf(d_) * 0.07216878364870322f); mn_ = fmaxf(mn_, s_[u]); } \
    const float al_ = __expf(m - mn_); m = mn_; l *= al_; \
    _Pragma("unroll") for (int i = 0; i < 24; ++i) acc[i] *= al_; \
    _Pragma("unroll") for (int u = 0; u < U; ++u) { const float p_ = __expf(s_[u] - mn_); l += p_; \
      _Pragma("unroll") for (int q_ = 0; q_ < 3; ++q_) _Pragma("unroll") for (int i = 0; i < 4; ++i) { \
        acc[q_ * 8 + 2 * i] = fmaf(p_, lo16(X[U + u][q_][i]), acc[q_ * 8 + 2 * i]); acc[q_ * 8 + 2 * i + 1] = fmaf(p_, hi16(X[U + u][q_][i]), acc[q_ * 8 + 2 * i + 1]); } } } while (0)
#define GRAB(gg) do { u64 sv_; asm volatile("s_mov_b64 %1, exec\n\ts_mov_b64 exec, 1\n\tglobal_atomic_add %0, %2, %3, %4 sc0\n\ts_mov_b64 exec, %1" : "=&v"(gg), "=&s"(sv_) : "v"(0u), "v"(1u), "s"(cp) : "memory"); } while (0)
    if (j >= 4 && DLY > 0) asm volatile("s_sleep %0" :: "i"(DLY) : "memory");
    LOADP(A, rnk, 0);
    if (j < 2) bar();
    LOADP(B, NW + rnk, 6 * U);
    LOADP(C, 2 * NW + rnk, 12 * U);
    const unsigned lbase = (unsigned)(L_KV + wv * KVW) * 4u;
#pragma unroll
    for (int t = 0; t < NL; ++t) {
      const unsigned so_ = TOFF((3 + t) * NW + rnk);
#pragma unroll
      for (int u = 0; u < 2 * U; ++u)
#pragma unroll
        for (int q_ = 0; q_ < 3; ++q_) {
          unsigned keep;
          asm volatile("s_waitcnt vmcnt(%6)\n\ts_mov_b32 %0, m0\n\ts_mov_b32 m0, %1\n\ts_nop 4\n\tbuffer_load_dwordx4 %2, %3, %4 offen nt lds\n\ts_mov_b32 m0, %0"
                       : "=&s"(keep) : "s"(lbase + (unsigned)((t * 2 * U + u) * 3 + q_) * 1024u), "v"(loff), "s"(u < U ? rk : rv),
                         "s"(so_ + (unsigned)((u % U) * 3072 + q_ * 128)), "i"(0), "i"(PF > 63 ? 63 : PF - 1) : "memory");
        }
    }
    if (wv == 0) STAMP(8);
    if (j >= 2) bar();
    u32x4 qv[3];
#pragma unroll
    for (int q_ = 0; q_ < 3; ++q_) qv[q_] = *reinterpret_cast<const u32x4*>(lds + L_Q + q_ * 32 + c * 4);
    if (wv == 0) STAMP(1);
    float m = -INFINITY, l = 0.f, acc[24];
#pragma unroll
    for (int i = 0; i < 24; ++i) acc[i] = 0.f;
    int tA = 0, tB = 0, tC = 0, n = 3 + NL;
    unsigned gA = 0, gB = 0, gC = 0;
#define NEXT(X, tX, gX, tP, gN) do { \
      asm volatile("s_waitcnt vmcnt(%1)" : "+v"(gX) : "i"(12 * U) : "memory"); \
      const int lim_ = (n & 1) ? TPG - ZEND : TPG;     \
      tX = tP >= lim_ ? TPG : n >= S ? S * NW + (int)__builtin_amdgcn_readfirstlane(gX) : n * NW + rnk; ++n; \
      if (n + 1 >= S && tX < lim_) GRAB(gN); \
      LOADT(X, tX); \
      asm volatile("" ::: "memory"); } while (0)
    do {
      if (tA < TPG) USE(A);
      NEXT(A, tA, gA, tB, gC);
      if (tB < TPG) USE(B);
      NEXT(B, tB, gB, tC, gA);
      if (tC < TPG) USE(C);
      if (NL > 0 && n == 5 + NL) {
        asm volatile("s_waitcnt vmcnt(%0)" :: "i"(12 * U) : "memory");
#pragma unroll 1
        for (int t = 0; t < NL; ++t) {
#pragma unroll
          for (int u = 0; u < 2 * U; ++u)
#pragma unroll
            for (int q_ = 0; q_ < 3; ++q_) C[u][q_] = *reinterpret_cast<const u32x4*>(lds + L_KV + wv * KVW + ((t * 2 * U + u) * 3 + q_) * 256 + lane * 4);
          USE(C);
        }
      }
      NEXT(C, tC, gC, tA, gB);
    } while (tA < TPG || tB < TPG || tC < TPG);
    const float M = max_hi3(m);
    const float sc = __expf(m - M);
    const float L = sum_hi3(l * sc);
    float y1[12], y2[6], y3[3];
#pragma unroll
    for (int k = 0; k < 12; ++k) y1[k] = tr32(acc[k] * sc, acc[k + 12] * sc);
#pragma unroll
    for (int k = 0; k < 6; ++k) y2[k] = tr16(y1[k], y1[k + 6]);
#pragma unroll
    for (int k = 0; k < 3; ++k) y3[k] = tr8(y2[k], y2[k + 3], (lane & 8) != 0);
    const int ai0 = 12 * (lane >> 5) + 6 * ((lane >> 4) & 1) + 3 * ((lane >> 3) & 1);
#pragma unroll
    for (int k = 0; k < 3; ++k) { const int ai = ai0 + k; ldf[L_PART + wv * 200 + (ai >> 3) * 64 + c * 8 + (ai & 7)] = y3[k]; }
    if (lane == 0) { ldf[L_PART + wv * 200 + 192] = M; ldf[L_PART + wv * 200 + 193] = L; }
    if (wv == 0) STAMP(2);
    bar();
    return;
  }

  const int vt = tid - 256, vw = wv - 4;
  float* wred = ldf + L_RED + vw * 480;
  const u64 E1 = (((u64)ev[1] << 32) | ev[0]) + 1ull;
  Tag tg; tg.tl = (unsigned)(E1 & 0xffffu) << 16; tg.th = (unsigned)(E1 >> 16);
  {
    const int ql = lane < 48 ? lane : 47;
    u32x4 g[2];
    gather<2>(rws, WS_P2 + (h * 2 * 192 + 48 * vw + ql) * 16, 192 * 16, tg, g);
    if (lane < 48) reinterpret_cast<u16*>(lds + L_Q)[48 * vw + lane] = (u16)bfbits(dec2(g[0]) + dec2(g[1]));
  }
  if (vw == 0) STAMP(14);
  bar();

  if (j == 4 || j == 5) {
    const int m2 = 2 * h + j - 4;
    const unsigned ob = lane < 50 ? 0u : OOB;
    u32x4 w[16]; unsigned cp2[8];
#pragma unroll
    for (int k = 0; k < 8; ++k) cp2[k] = __builtin_amdgcn_raw_buffer_load_b32(rc, (160 * vw + 2 * (mrg + 10 * k)) * 2 | ob, 0, 0);
#pragma unroll
    for (int k = 0; k < 16; ++k) w[k] = __builtin_amdgcn_raw_buffer_load_b128(rmod, ((160 * vw + 2 * (mrg + 10 * (k >> 1)) + (k & 1)) * (6 * D) + 2 * D + 40 * m2 + 8 * mcc) * 2 | ob, 0, AUX_NT);
    float a8[8];
#pragma unroll
    for (int i = 0; i < 8; ++i) a8[i] = 0.f;
#pragma unroll
    for (int k = 0; k < 8; ++k) {
      const unsigned sp = bfbits(silu_f(lo16(cp2[k]))) | (bfbits(silu_f(hi16(cp2[k]))) << 16);
#pragma unroll
      for (int i = 0; i < 4; ++i) { a8[2 * i] = dot2(plo(w[2 * k + 1][i], w[2 * k][i]), sp, a8[2 * i]); a8[2 * i + 1] = dot2(phi(w[2 * k + 1][i], w[2 * k][i]), sp, a8[2 * i + 1]); }
    }
    if (lane < 50) {
      *reinterpret_cast<float4*>(wred + mrg * 40 + mcc * 8) = float4{a8[0], a8[1], a8[2], a8[3]};
      *reinterpret_cast<float4*>(wred + mrg * 40 + mcc * 8 + 4) = float4{a8[4], a8[5], a8[6], a8[7]};
    }
    lds_wait();
    unsigned old = 0;
    if (lane == 0) old = __hip_atomic_fetch_add(lds + L_CNT + 1, 1u, __ATOMIC_RELAXED, __HIP_MEMORY_SCOPE_WORKGROUP);
    old = __builtin_amdgcn_readfirstlane(old);
    asm volatile("" ::: "memory");
    if (old == 3u) {
      float p4[4] = {0.f, 0.f, 0.f, 0.f};
#pragma unroll
      for (int g = 0; g < 10; ++g)
#pragma unroll
        for (int w4 = 0; w4 < 4; ++w4) p4[w4] += ldf[L_RED + w4 * 480 + g * 40 + gl];
      if (lane < 40) st8(rws, WS_M2 + (40 * m2 + lane) * 8, u32x2{bfbits((p4[0] + p4[1]) + (p4[2] + p4[3])) | tg.tl, tg.th});
      lds_wait();
      if (lane == 0) __hip_atomic_store(lds + L_CNT + 1, 8u, __ATOMIC_RELAXED, __HIP_MEMORY_SCOPE_WORKGROUP);
    }
    for (unsigned spin = 0; spin < (SPIN_LIMIT << 4); ++spin) {
      if (__hip_atomic_load(lds + L_CNT + 1, __ATOMIC_RELAXED, __HIP_MEMORY_SCOPE_WORKGROUP) >= 8u) break;
      asm volatile("s_sleep 2" ::: "memory");
    }
    asm volatile("" ::: "memory");
  }
  const int org = lane / 10, ocg = lane - 10 * org;
  unsigned wop[4][8];
  {
    const rsrc_t rwo = mk(wo, HD * D * 2);
    const unsigned o0 = lane < 60 ? ((h * DH + 48 * vw + 8 * org) * D + 80 * j + 8 * ocg) * 2 : OOB;
    u32x4 w[8];
#pragma unroll
    for (int k = 0; k < 8; ++k) w[k] = __builtin_amdgcn_raw_buffer_load_b128(rwo, o0, k * D * 2, AUX_NT);
#pragma unroll
    for (int p = 0; p < 4; ++p)
#pragma unroll
      for (int i = 0; i < 4; ++i) { wop[p][2 * i] = plo(w[2 * p + 1][i], w[2 * p][i]); wop[p][2 * i + 1] = phi(w[2 * p + 1][i], w[2 * p][i]); }
  }
  auto h1_stage = [&]() {
    bar();
    if (vw == 0) STAMP(4);
    {
      const float m0 = ldf[L_PART + 192], m1 = ldf[L_PART + 200 + 192], m2 = ldf[L_PART + 400 + 192], m3 = ldf[L_PART + 600 + 192];
      const float M = fmaxf(fmaxf(m0, m1), fmaxf(m2, m3));
      const float e0 = __expf(m0 - M), e1 = __expf(m1 - M), e2 = __expf(m2 - M), e3 = __expf(m3 - M);
      const int idx = vt < 192 ? vt : 193;
      float v = e0 * ldf[L_PART + idx];
      v = fmaf(e1, ldf[L_PART + 200 + idx], v); v = fmaf(e2, ldf[L_PART + 400 + idx], v); v = fmaf(e3, ldf[L_PART + 600 + idx], v);
      if (vt == 192) v = M;
      if (vt < 194) st16(rws, WS_R + (b * 200 + vt) * 16, enc2(v, tg));
    }
    {
      const int s = lane >> 3, dg = lane & 7;
      u32x4 g[8];
      const unsigned rb = WS_R + (h * 8 + s) * 200 * 16, r0 = rb + (48 * vw + dg) * 16;
      poll<8>([&](int k) { return __builtin_amdgcn_raw_buffer_load_b128(rws, k < 6 ? r0 + k * 128 : rb + (186 + k) * 16, 0, AUX_SC1); }, tg, g);
      if (vw == 0) STAMP(15);
      const float ms = dec2(g[6]), M = max_hi3(ms), e = __expf(ms - M);
      const float L = sum_hi3(e * dec2(g[7])), rl = __builtin_amdgcn_rcpf(L);
      float z[3];
#pragma unroll
      for (int k = 0; k < 3; ++k) z[k] = tr32(e * dec2(g[k]), e * dec2(g[k + 3]));
      const float z01 = tr16(z[0], z[1]), z2 = tr16(z[2], z[2]);
      const float ov = tr8(z01, z2, (lane & 8) != 0);
      u16* op = reinterpret_cast<u16*>(lds + L_OP + vw * 24) + dg + 24 * (lane >> 5);
      if ((lane & 24) != 24) op[(lane & 8) ? 16 : 8 * ((lane >> 4) & 1)] = (u16)bfbits(fdiv(ov, L, rl));
      lds_wait();
      const u32x4 opv = *reinterpret_cast<const u32x4*>(lds + L_OP + vw * 24 + 4 * (org < 6 ? org : 0));
      float y[8];
#pragma unroll
      for (int i = 0; i < 8; ++i) y[i] = 0.f;
#pragma unroll
      for (int p = 0; p < 4; ++p)
#pragma unroll
        for (int i = 0; i < 8; ++i) y[i] = dot2(wop[p][i], opv[p], y[i]);
      if (lane < 60) {
        float* dst = ldf + L_RED + (vw * 6 + org) * 80 + 8 * ocg;
        *reinterpret_cast<float4*>(dst) = float4{y[0], y[1], y[2], y[3]};
        *reinterpret_cast<float4*>(dst + 4) = float4{y[4], y[5], y[6], y[7]};
      }
    }
    bar();
    if (vw < 3) {
      const int yc = vt < 160 ? vt >> 1 : 79;
      const float* src = ldf + L_RED + (vt & 1) * 960 + yc;
      float p4[4] = {0.f, 0.f, 0.f, 0.f};
#pragma unroll
      for (int g = 0; g < 12; ++g) p4[g & 3] += src[g * 80];
      float y = (p4[0] + p4[1]) + (p4[2] + p4[3]);
      y += dpp<XOR1>(y);
      if (vt < 160 && (vt & 1) == 0) st16(rws, WS_Y + (((20 * j + (yc >> 2)) * 32 + h) * 4 + (yc & 3)) * 16, enc2(y, tg));
    }
    if (vw == 0) STAMP(5);
  };

  if (j == 2 || j == 3) {
    const int f = 2 * h + j - 2, rg = vt >> 3, cc = vt & 7;
    if (vw < 3) {
      const rsrc_t rwd = mk(wd, F * D * 2);
      const unsigned o0 = vt < 160 ? (24 * f * D + 4 * vt) * 2 : OOB;
      u32x2 w[24];
#pragma unroll
      for (int k = 0; k < 24; ++k) w[k] = __builtin_amdgcn_raw_buffer_load_b64(rwd, o0, k * D * 2, AUX_NT);
      if (vt < 160) {
#pragma unroll
        for (int p = 0; p < 12; ++p)
          *reinterpret_cast<u32x4*>(lds + L_WD + (p * 160 + vt) * 4) =
              u32x4{plo(w[2 * p + 1][0], w[2 * p][0]), phi(w[2 * p + 1][0], w[2 * p][0]), plo(w[2 * p + 1][1], w[2 * p][1]), phi(w[2 * p + 1][1], w[2 * p][1])};
      }
    }
    unsigned gp[10][8];
    {
      const rsrc_t rg_ = mk(wg, D * F * 2), ru_ = mk(wu, D * F * 2);
      const unsigned og = cc < 3 ? (20 * rg * F + 24 * f + 8 * cc) * 2 : OOB, ou = (cc >= 3 && cc < 6) ? (20 * rg * F + 24 * f + 8 * (cc - 3)) * 2 : OOB;
#pragma unroll
      for (int hf = 0; hf < 2; ++hf) {
        u32x4 w[10], w2[10];
#pragma unroll
        for (int k = 0; k < 10; ++k) {
          w[k] = __builtin_amdgcn_raw_buffer_load_b128(rg_, og, (10 * hf + k) * F * 2, AUX_NT);
          w2[k] = __builtin_amdgcn_raw_buffer_load_b128(ru_, ou, (10 * hf + k) * F * 2, AUX_NT);
        }
#pragma unroll
        for (int k = 0; k < 10; ++k) w[k] |= w2[k];
#pragma unroll
        for (int p = 0; p < 5; ++p)
#pragma unroll
          for (int i = 0; i < 4; ++i) { gp[5 * hf + p][2 * i] = plo(w[2 * p + 1][i], w[2 * p][i]); gp[5 * hf + p][2 * i + 1] = phi(w[2 * p + 1][i], w[2 * p][i]); }
      }
#pragma unroll
      for (int p = 0; p < 10; ++p)
#pragma unroll
        for (int i = 0; i < 8; ++i) asm volatile("" : "+v"(gp[p][i]));
    }
    unsigned po[5];
#pragma unroll
    for (int k = 0; k < 5; ++k) { const int p = 80 * vw + lane + 64 * k; po[k] = (unsigned)(p >= 320 ? p - 320 : p) * 16u; }
    float sbv[4], c1v[4];
    {
      u32x4 g[4];
      const unsigned off[4] = {WS_M2 + D * 8 + po[0], WS_M2 + D * 8 + po[1], WS_M2 + 2 * D * 8 + po[0], WS_M2 + 2 * D * 8 + po[1]};
      gather_slow<4>(rws, off, tg, g);
#pragma unroll
      for (int k = 0; k < 2; ++k) {
        const unsigned sp = decp(g[k]), cq = decp(g[2 + k]);
        sbv[2 * k] = lo16(sp); sbv[2 * k + 1] = hi16(sp); c1v[2 * k] = rbf(1.f + lo16(cq)); c1v[2 * k + 1] = rbf(1.f + hi16(cq));
      }
    }
    h1_stage();
    u32x4 wdr[12];
#pragma unroll
    for (int p = 0; p < 12; ++p) wdr[p] = *reinterpret_cast<const u32x4*>(lds + L_WD + (p * 160 + (vt < 160 ? vt : 0)) * 4);
    {
      u32x4 g[5];
      poll<5>([&](int k) { return __builtin_amdgcn_raw_buffer_load_b128(rws, WS_X1 + po[k], 0, AUX_SC1); }, tg, g);
      if (vw == 0) STAMP(6);
      float xv[10];
#pragma unroll
      for (int k = 0; k < 5; ++k) { xv[2 * k] = __uint_as_float(g[k][0] << 16); xv[2 * k + 1] = __uint_as_float(g[k][2] << 16); }
      const float s = ((xv[0] + xv[1]) + (xv[2] + xv[3])) + ((xv[4] + xv[5]) + (xv[6] + xv[7])) + (xv[8] + xv[9]);
      const float mean = sum_wave(s) * (1.f / D);
      float v0 = 0.f, v1 = 0.f;
#pragma unroll
      for (int k = 0; k < 5; ++k) { const float d0 = xv[2 * k] - mean, d1 = xv[2 * k + 1] - mean; v0 = fmaf(d0, d0, v0); v1 = fmaf(d1, d1, v1); }
      const float var = sum_wave(v0 + v1) * (1.f / D);
      const float mb2 = rbf(mean), den2 = rbf(sqrtf(rbf(rbf(var) + 1e-5f))), rd = __builtin_amdgcn_rcpf(den2);
#pragma unroll
      for (int k = 0; k < 2; ++k) {
        const float n0 = rbf(fdiv(rbf(xv[2 * k] - mb2), den2, rd)), n1 = rbf(fdiv(rbf(xv[2 * k + 1] - mb2), den2, rd));
        const float h0 = rbf(n0 * c1v[2 * k]) + sbv[2 * k], h1 = rbf(n1 * c1v[2 * k + 1]) + sbv[2 * k + 1];
        if (k == 0 || lane < 16) lds[L_H2 + vw * 80 + lane + 64 * k] = bfbits(h0) | (bfbits(h1) << 16);
      }
    }
    lds_wait();
    {
      float a8[8];
#pragma unroll
      for (int i = 0; i < 8; ++i) a8[i] = 0.f;
#pragma unroll
      for (int p2 = 0; p2 < 5; ++p2) {
        const u32x2 hp = *reinterpret_cast<const u32x2*>(lds + L_H2 + vw * 80 + 10 * (lane >> 3) + 2 * p2);
#pragma unroll
        for (int i = 0; i < 8; ++i) { a8[i] = dot2(gp[2 * p2][i], hp[0], a8[i]); a8[i] = dot2(gp[2 * p2 + 1][i], hp[1], a8[i]); }
      }
      float z1[4], z2[2];
#pragma unroll
      for (int k = 0; k < 4; ++k) z1[k] = tr32(a8[k], a8[k + 4]);
#pragma unroll
      for (int k = 0; k < 2; ++k) z2[k] = tr16(z1[k], z1[k + 2]);
      const float z3 = tr8(z2[0], z2[1], (lane & 8) != 0);
      ldf[L_GU + vw * 64 + cc * 8 + 4 * (lane >> 5) + 2 * ((lane >> 4) & 1) + ((lane >> 3) & 1)] = z3;
    }
    bar();
    if (vw < 3) {
      const int n = lane < 24 ? lane : 0;
      const float g = (ldf[L_GU + n] + ldf[L_GU + 64 + n]) + (ldf[L_GU + 128 + n] + ldf[L_GU + 192 + n]);
      const float u = (ldf[L_GU + 24 + n] + ldf[L_GU + 88 + n]) + (ldf[L_GU + 152 + n] + ldf[L_GU + 216 + n]);
      const unsigned ab = bfbits(rbf(silu_f(rbf(g))) * rbf(u));
      const unsigned nb = (unsigned)__builtin_amdgcn_mov_dpp((int)ab, XOR1, 0xf, 0xf, true);
      const unsigned ap = ab | (nb << 16);
      float y[4] = {0.f, 0.f, 0.f, 0.f};
#pragma unroll
      for (int p = 0; p < 12; ++p) {
        const unsigned a2 = (unsigned)__builtin_amdgcn_readlane((int)ap, 2 * p);
#pragma unroll
        for (int i = 0; i < 4; ++i) y[i] = dot2(wdr[p][i], a2, y[i]);
      }
      const bool o1 = (lane & 1) != 0, o2 = (lane & 2) != 0;
      { const float s0 = o1 ? y[0] : y[1], s1 = o1 ? y[2] : y[3]; const float r0 = dpp<XOR1>(s0), r1 = dpp<XOR1>(s1);
        if (o1) { y[0] = r0; y[2] = r1; } else { y[1] = r0; y[3] = r1; } }
      { const float s0 = o2 ? y[0] : y[2], s1 = o2 ? y[1] : y[3]; const float r0 = dpp<XOR2>(s0), r1 = dpp<XOR2>(s1);
        if (o2) { y[0] = r0; y[1] = r1; } else { y[2] = r0; y[3] = r1; } }
      if (vt < 160) {
#pragma unroll
        for (int i = 0; i < 4; ++i) st16(rws, WS_O + ((((vt & ~3) + i) * NFFN + f) * 4 + (vt & 3)) * 16, enc2(y[i], tg));
      }
    }
    if (vw == 0) STAMP(10);
  } else {
    const bool red = j != 4 && vw == 3;
    const int rr = 5 * h + (j < 2 ? j : j - 3), col = 4 * rr + (lane & 3);
    float ga = 0.f, gb = 0.f, xv = 0.f;
    if (red) {
      u32x4 g[2];
      const unsigned off[2] = {WS_M2 + (col & ~1) * 8, WS_M2 + (col & ~1) * 8 + 3 * D * 8};
      gather_slow<2>(rws, off, tg, g);
      const unsigned pa = decp(g[0]), pb = decp(g[1]);
      ga = (col & 1) ? hi16(pa) : lo16(pa); gb = (col & 1) ? hi16(pb) : lo16(pb);
      xv = __uint_as_float((unsigned)x[col] << 16);
    }
    h1_stage();
    if (red) {
      float x1;
      {
        u32x4 g[2];
        gather<2>(rws, WS_Y + (rr * 128 + lane) * 16, 64 * 16, tg, g);
        float y = dec2(g[0]) + dec2(g[1]);
        y += dpp<ROR4>(y); y = sum_hi3(y);
        x1 = rbf(xv + rbf(ga * rbf(y)));
        if (lane < 4) st8(rws, WS_X1 + col * 8, u32x2{bfbits(x1) | tg.tl, tg.th});
      }
      STAMP(3);
      {
        u32x4 g[4];
        gather<4>(rws, WS_O + (rr * NFFN * 4 + lane) * 16, 64 * 16, tg, g);
        float f = (dec2(g[0]) + dec2(g[1])) + (dec2(g[2]) + dec2(g[3]));
        f += dpp<ROR4>(f); f = sum_hi3(f);
        const float o = x1 + rbf(gb * rbf(f));
        if (lane < 4) out[col] = (u16)bfbits(o);
        if (b == 0 && lane == 0) *reinterpret_cast<u64*>(ws + WS_EPOCH) = E1;
      }
      STAMP(7);
    }
  }
}

extern "C" void kernel_launch(void* const* d_in, const int* in_sizes, int n_in, void* d_out, int out_size, void* d_ws, size_t ws_size, hipStream_t stream) {
  (void)in_sizes; (void)n_in; (void)out_size; (void)ws_size;
  k_block<<<NWG, 512, 0, stream>>>((const u16*)d_in[0], (const u16*)d_in[1], (const u16*)d_in[2], (const u16*)d_in[3], (const u16*)d_in[4],
                                   (const u16*)d_in[6], (const u16*)d_in[7], (const u16*)d_in[8], (const u16*)d_in[9], (const u16*)d_in[10],
                                   (u16*)d_out, (unsigned char*)d_ws);
}
```

```cpp
#include <hip/hip_runtime.h>
#include <cstdint>

typedef unsigned short u16;
typedef unsigned long long u64;
using u32x4 = __attribute__((ext_vector_type(4))) unsigned;
using u32x2 = __attribute__((ext_vector_type(2))) unsigned;
using bf16x2 = __attribute__((ext_vector_type(2))) __bf16;

#define KU 2
#define SPCT 75
#define PF 12
#define NL 2
#define PSLEEP 10
#define ZEND 192
#define PRIO 2
#define ROT 11
#define STAMPS 0

constexpr int D = 640, H = 32, SK = 32768, DH = 192, F = 1536, HD = H * DH;
constexpr int NWG = 256, NFFN = 64;
constexpr unsigned WS_EPOCH = 0;
constexpr unsigned WS_CTR = 1024;
constexpr unsigned WS_P1 = 16384;
constexpr unsigned WS_P2 = WS_P1 + 2 * 1280 * 16;
constexpr unsigned WS_M2 = WS_P2 + 32 * 2 * 192 * 16;
constexpr unsigned WS_R = WS_M2 + 2560 * 8;
constexpr unsigned WS_Y = WS_R + 256 * 200 * 16;
constexpr unsigned WS_X1 = WS_Y + 160 * 32 * 64;
constexpr unsigned WS_O = WS_X1 + 640 * 8;
constexpr unsigned WS_ST = WS_O + 160 * NFFN * 64;
constexpr unsigned WS_END = WS_ST + 256 * 16 * 8;

__device__ __forceinline__ float rbf(float x) { return (float)(__bf16)x; }
__device__ __forceinline__ unsigned bfbits(float x) { return (unsigned)__builtin_bit_cast(u16, (__bf16)x); }
__device__ __forceinline__ float lo16(unsigned v) { return __uint_as_float(v << 16); }
__device__ __forceinline__ float hi16(unsigned v) { return __uint_as_float(v & 0xffff0000u); }
__device__ __forceinline__ float dot2(unsigned a, unsigned b, float acc) {
  return __builtin_amdgcn_fdot2_f32_bf16(__builtin_bit_cast(bf16x2, a), __builtin_bit_cast(bf16x2, b), acc, false);
}
template <int CTRL> __device__ __forceinline__ float dpp(float x) {
  return __builtin_bit_cast(float, __builtin_amdgcn_mov_dpp(__builtin_bit_cast(int, x), CTRL, 0xf, 0xf, true));
}
constexpr int XOR1 = 0xB1, XOR2 = 0x4E, HMIR = 0x141, ROR8 = 0x128, ROR4 = 0x124;
__device__ __forceinline__ float tr32(float a, float b) {
  auto s = __builtin_amdgcn_permlane32_swap(__float_as_uint(a), __float_as_uint(b), false, false);
  return __uint_as_float(s[0]) + __uint_as_float(s[1]);
}
__device__ __forceinline__ float tr16(float a, float b) {
  auto s = __builtin_amdgcn_permlane16_swap(__float_as_uint(a), __float_as_uint(b), false, false);
  return __uint_as_float(s[0]) + __uint_as_float(s[1]);
}
__device__ __forceinline__ float tr8(float a, float b, bool bit3) { const float t1 = a + dpp<ROR8>(a), t2 = b + dpp<ROR8>(b); return bit3 ? t2 : t1; }
__device__ __forceinline__ float mx32(float a) { auto s = __builtin_amdgcn_permlane32_swap(__float_as_uint(a), __float_as_uint(a), false, false); return fmaxf(__uint_as_float(s[0]), __uint_as_float(s[1])); }
__device__ __forceinline__ float mx16(float a) { auto s = __builtin_amdgcn_permlane16_swap(__float_as_uint(a), __float_as_uint(a), false, false); return fmaxf(__uint_as_float(s[0]), __uint_as_float(s[1])); }
__device__ __forceinline__ float sum_hi3(float a) { a += dpp<ROR8>(a); a = tr16(a, a); return tr32(a, a); }
__device__ __forceinline__ float max_hi3(float a) { a = fmaxf(a, dpp<ROR8>(a)); return mx32(mx16(a)); }
__device__ __forceinline__ float sum_row16(float a) { a += dpp<XOR1>(a); a += dpp<XOR2>(a); a += dpp<HMIR>(a); a += dpp<ROR8>(a); return a; }
__device__ __forceinline__ float sum_wave(float a) { a = sum_row16(a); a = tr16(a, a); return tr32(a, a); }
__device__ __forceinline__ unsigned plo(unsigned r1, unsigned r0) { return __builtin_amdgcn_perm(r1, r0, 0x05040100u); }
__device__ __forceinline__ unsigned phi(unsigned r1, unsigned r0) { return __builtin_amdgcn_perm(r1, r0, 0x07060302u); }
__device__ __forceinline__ float silu_f(float x) { return x / (1.f + __expf(-x)); }
__device__ __forceinline__ void bar() { asm volatile("s_waitcnt lgkmcnt(0)" ::: "memory"); __builtin_amdgcn_s_barrier(); asm volatile("" ::: "memory"); }

typedef __amdgpu_buffer_rsrc_t rsrc_t;
__device__ __forceinline__ rsrc_t mk(const void* p, unsigned bytes) { return __builtin_amdgcn_make_buffer_rsrc((void*)p, 0, bytes, 0x00020000); }
constexpr int AUX_NT = 2, AUX_SC1 = 16;

struct Tag { unsigned tl, th; };
__device__ __forceinline__ u32x4 enc2(float v, Tag t) { const unsigned b = __float_as_uint(v); return u32x4{(b & 0xffffu) | t.tl, t.th, (b >> 16) | t.tl, t.th}; }
__device__ __forceinline__ float dec2(u32x4 g) { return __uint_as_float((g[0] & 0xffffu) | (g[2] << 16)); }
__device__ __forceinline__ unsigned decp(u32x4 g) { return (g[0] & 0xffffu) | (g[2] << 16); }
__device__ __forceinline__ void st16(rsrc_t r, unsigned off, u32x4 v) { __builtin_amdgcn_raw_buffer_store_b128(v, r, off, 0, AUX_SC1); }
__device__ __forceinline__ void st8(rsrc_t r, unsigned off, u32x2 v) { __builtin_amdgcn_raw_buffer_store_b64(v, r, off, 0, AUX_SC1); }

constexpr unsigned SPIN_LIMIT = 1u << 19;
constexpr unsigned OOB = 0x80000000u;
__device__ __forceinline__ unsigned bad2(u32x4 g, Tag t) { return (((g[0] ^ t.tl) | (g[2] ^ t.tl)) >> 16) | (g[1] ^ t.th) | (g[3] ^ t.th); }
template <int N, class LD>
__device__ __forceinline__ void poll(LD ld, Tag t, u32x4 (&g)[N]) {
  u32x4 y[N];
#pragma unroll
  for (int k = 0; k < N; ++k) g[k] = ld(k);
  asm volatile("s_sleep %0" :: "i"(PSLEEP) : "memory");
  for (unsigned spin = 0; spin < SPIN_LIMIT; ++spin) {
#pragma unroll
    for (int k = 0; k < N; ++k) y[k] = ld(k);
    unsigned bad = 0;
#pragma unroll
    for (int k = 0; k < N; ++k) bad |= bad2(g[k], t);
    if (__builtin_amdgcn_ballot_w64(bad != 0u) == 0ull) break;
    asm volatile("" ::: "memory");
#pragma unroll
    for (int k = 0; k < N; ++k) g[k] = ld(k);
    bad = 0;
#pragma unroll
    for (int k = 0; k < N; ++k) bad |= bad2(y[k], t);
    if (__builtin_amdgcn_ballot_w64(bad != 0u) == 0ull) {
#pragma unroll
      for (int k = 0; k < N; ++k) g[k] = y[k];
      break;
    }
    asm volatile("" ::: "memory");
  }
}
template <int N>
__device__ __forceinline__ void gather(rsrc_t r, unsigned off, unsigned stride, Tag t, u32x4 (&g)[N]) {
  poll<N>([&](int k) { return __builtin_amdgcn_raw_buffer_load_b128(r, off + k * stride, 0, AUX_SC1); }, t, g);
}
template <int N>
__device__ __forceinline__ void gather_slow(rsrc_t r, const unsigned (&off)[N], Tag t, u32x4 (&g)[N]) {
  for (unsigned spin = 0; spin < SPIN_LIMIT; ++spin) {
#pragma unroll
    for (int k = 0; k < N; ++k) g[k] = __builtin_amdgcn_raw_buffer_load_b128(r, off[k], 0, AUX_SC1);
    unsigned bad = 0;
#pragma unroll
    for (int k = 0; k < N; ++k) bad |= bad2(g[k], t);
    if (__builtin_amdgcn_ballot_w64(bad != 0u) == 0ull) break;
    asm volatile("s_sleep 16" ::: "memory");
  }
}
__device__ __forceinline__ void lds_wait() { asm volatile("s_waitcnt lgkmcnt(0)" ::: "memory"); }
__device__ __forceinline__ float fdiv(float a, float d, float r) { const float q = a * r; return fmaf(fmaf(-q, d, a), r, q); }

#define STAMP(i) do { } while (0)

constexpr int L_RED = 0;
constexpr int L_HS = L_RED + 1920;
constexpr int L_Q = L_HS + 160;
constexpr int L_PART = L_Q + 96;
constexpr int L_OP = L_PART + 800;
constexpr int L_H2 = L_OP + 96;
constexpr int L_GU = L_H2 + 320;
constexpr int L_CNT = L_GU + 256;
constexpr int L_WD = L_CNT + 4;
constexpr int L_KV = L_WD + 7680;
constexpr int KVW = (NL > 0 ? NL : 1) * KU * 1536;
constexpr int L_TOTAL = L_KV + 4 * KVW < 24576 ? 24576 : L_KV + 4 * KVW;
static_assert(L_WD % 4 == 0 && L_KV % 4 == 0 && L_TOTAL * 4 <= 163840, "lds map");

__global__ void __launch_bounds__(512, 2)
k_block(const u16* __restrict__ x, const u16* __restrict__ Kc, const u16* __restrict__ Vc, const u16* __restrict__ wq, const u16* __restrict__ wo,
        const u16* __restrict__ wg, const u16* __restrict__ wu, const u16* __restrict__ wd, const u16* __restrict__ cnd, const u16* __restrict__ wmod,
        u16* __restrict__ out, unsigned char* __restrict__ ws) {
  __shared__ __attribute__((aligned(16))) unsigned lds[L_TOTAL];
  float* ldf = reinterpret_cast<float*>(lds);
  const int tid = threadIdx.x, lane = tid & 63, wv = __builtin_amdgcn_readfirstlane(tid >> 6);
  const int b = blockIdx.x, h = b >> 3, j = b & 7;
  const rsrc_t rws = mk(ws, WS_END);
  const rsrc_t rmod = mk(wmod, D * 6 * D * 2), rc = mk(cnd, D * 2);
  const int mrg = lane / 5, mcc = lane - 5 * mrg;
  const int gl = lane < 40 ? lane : 39;
  if (wv == 4) STAMP(0);
  const u32x2 ev = __builtin_amdgcn_raw_buffer_load_b64(rws, WS_EPOCH, 0, AUX_SC1);
  if (tid == 511) { lds[L_CNT] = 0u; lds[L_CNT + 1] = 0u; }

  if (j == 2 || j == 3) {
    const int kb = j - 2;
    u32x4 wm[4]; unsigned cpair[2];
    const unsigned ob = lane < 50 ? 0u : OOB;
#pragma unroll
    for (int k = 0; k < 4; ++k) wm[k] = __builtin_amdgcn_raw_buffer_load_b128(rmod, ((320 * kb + 40 * wv + 2 * (mrg + 10 * (k >> 1)) + (k & 1)) * (6 * D) + 40 * h + 8 * mcc) * 2 | ob, 0, AUX_NT);
#pragma unroll
    for (int k = 0; k < 2; ++k) cpair[k] = __builtin_amdgcn_raw_buffer_load_b32(rc, (320 * kb + 40 * wv + 2 * (mrg + 10 * k)) * 2 | ob, 0, 0);
    float a8[8];
#pragma unroll
    for (int i = 0; i < 8; ++i) a8[i] = 0.f;
#pragma unroll
    for (int k = 0; k < 2; ++k) {
      const unsigned sp = bfbits(silu_f(lo16(cpair[k]))) | (bfbits(silu_f(hi16(cpair[k]))) << 16);
#pragma unroll
      for (int i = 0; i < 4; ++i) { a8[2 * i] = dot2(plo(wm[2 * k + 1][i], wm[2 * k][i]), sp, a8[2 * i]); a8[2 * i + 1] = dot2(phi(wm[2 * k + 1][i], wm[2 * k][i]), sp, a8[2 * i + 1]); }
    }
    if (lane < 50) {
      float* d_ = ldf + L_WD + (wv * 10 + mrg) * 40 + mcc * 8;
      *reinterpret_cast<float4*>(d_) = float4{a8[0], a8[1], a8[2], a8[3]};
      *reinterpret_cast<float4*>(d_ + 4) = float4{a8[4], a8[5], a8[6], a8[7]};
    }
    const u64 E1 = (((u64)ev[1] << 32) | ev[0]) + 1ull;
    Tag tg; tg.tl = (unsigned)(E1 & 0xffffu) << 16; tg.th = (unsigned)(E1 >> 16);
    bar();
    if (wv >= 3) {
      const int t = tid - 192;
      const float* src = ldf + L_WD + (t & 7) * 400 + (t >> 3);
      float s0 = 0.f, s1 = 0.f;
#pragma unroll
      for (int g = 0; g < 5; ++g) { s0 += src[g * 40]; s1 += src[(g + 5) * 40]; }
      float s = s0 + s1;
      s += dpp<XOR1>(s); s += dpp<XOR2>(s); s += dpp<HMIR>(s);
      if ((t & 7) == 0) st16(rws, WS_P1 + (kb * 1280 + 40 * h + (t >> 3)) * 16, enc2(s, tg));
      if (wv == 4) STAMP(11);
    }
    bar();
  } else if (j < 2) {
    unsigned xp[5];
    if (j == 0 && tid == 0) (void)__hip_atomic_exchange((unsigned*)(ws + WS_CTR + h * 256), 0u, __ATOMIC_RELAXED, __HIP_MEMORY_SCOPE_AGENT);
#pragma unroll
    for (int k = 0; k < 5; ++k) xp[k] = *reinterpret_cast<const unsigned*>(x + 2 * (lane + 64 * k));
    const int hr = tid < 320 ? tid : 319;
    const float xi = __uint_as_float((unsigned)x[320 * j + hr] << 16);
    asm volatile("" ::: "memory");
    const int qr = lane / 12, qc = lane - 12 * qr;
    u32x4 wqr[16];
    {
      const rsrc_t rwq = mk(wq, D * HD * 2);
      const unsigned o0 = lane < 60 ? ((320 * j + 80 * (wv >> 1) + 2 * qr) * HD + h * DH + 96 * (wv & 1) + 8 * qc) * 2 : OOB;
#pragma unroll
      for (int k = 0; k < 16; ++k) wqr[k] = __builtin_amdgcn_raw_buffer_load_b128(rwq, o0, (10 * (k >> 1) + (k & 1)) * HD * 2, AUX_NT);
    }
    asm volatile("" ::: "memory");
    const u64 E1 = (((u64)ev[1] << 32) | ev[0]) + 1ull;
    Tag tg; tg.tl = (unsigned)(E1 & 0xffffu) << 16; tg.th = (unsigned)(E1 >> 16);
    if (wv < 5) {
      float sx = 0.f;
#pragma unroll
      for (int k = 0; k < 5; ++k) sx += lo16(xp[k]) + hi16(xp[k]);
      const float mean = sum_wave(sx) * (1.f / D);
      float v = 0.f;
#pragma unroll
      for (int k = 0; k < 5; ++k) { const float d0 = lo16(xp[k]) - mean, d1 = hi16(xp[k]) - mean; v = fmaf(d0, d0, v); v = fmaf(d1, d1, v); }
      const float var = sum_wave(v) * (1.f / D);
      const float mb = rbf(mean), den = rbf(sqrtf(rbf(rbf(var) + 1e-5f)));
      float nrm = rbf(rbf(xi - mb) / den);
      asm volatile("" : "+v"(nrm));
      u32x4 g[4];
      const unsigned p0 = WS_P1 + (320 * j + tid) * 16;
      poll<4>([&](int k) { return __builtin_amdgcn_raw_buffer_load_b128(rws, p0 + (k & 1) * 1280 * 16 + (k >> 1) * 640 * 16, 0, AUX_SC1); }, tg, g);
      const float sa = dec2(g[0]) + dec2(g[1]), ca = dec2(g[2]) + dec2(g[3]);
      const float hv = rbf(nrm * rbf(1.f + rbf(ca))) + rbf(sa);
      reinterpret_cast<u16*>(lds + L_HS)[tid] = (u16)bfbits(hv);
      if (wv == 0) STAMP(12);
    }
    unsigned wqp[8][8];
#pragma unroll
    for (int k = 0; k < 8; ++k)
#pragma unroll
      for (int i = 0; i < 4; ++i) { wqp[k][2 * i] = plo(wqr[2 * k + 1][i], wqr[2 * k][i]); wqp[k][2 * i + 1] = phi(wqr[2 * k + 1][i], wqr[2 * k][i]); }
    asm volatile("s_waitcnt vmcnt(0)" ::: "memory");
    bar();
    {
      float a8[8];
#pragma unroll
      for (int i = 0; i < 8; ++i) a8[i] = 0.f;
#pragma unroll
      for (int k = 0; k < 8; ++k) {
        const unsigned hp = lds[L_HS + 40 * (wv >> 1) + (qr < 5 ? qr : 0) + 5 * k];
#pragma unroll
        for (int i = 0; i < 8; ++i) a8[i] = dot2(wqp[k][i], hp, a8[i]);
      }
      if (lane < 60) {
        float* d_ = ldf + L_WD + 3200 + ((wv >> 1) * 5 + qr) * 192 + 96 * (wv & 1) + qc * 8;
        *reinterpret_cast<float4*>(d_) = float4{a8[0], a8[1], a8[2], a8[3]};
        *reinterpret_cast<float4*>(d_ + 4) = float4{a8[4], a8[5], a8[6], a8[7]};
      }
    }
    bar();
    if (wv >= 2) {
      const int t = tid - 128;
      const float* src = ldf + L_WD + 3200 + (t & 1) * 1920 + (t >> 1);
      float s0 = 0.f, s1 = 0.f;
#pragma unroll
      for (int g = 0; g < 5; ++g) { s0 += src[g * 192]; s1 += src[(g + 5) * 192]; }
      float s = s0 + s1;
      s += dpp<XOR1>(s);
      if ((t & 1) == 0) st16(rws, WS_P2 + ((h * 2 + j) * 192 + (t >> 1)) * 16, enc2(s, tg));
      if (wv == 4) STAMP(13);
    }
    bar();
  }

  if (wv < 4) {
    constexpr int U = KU;
    constexpr int NW = 32;
    constexpr int TPG = SK / (8 * U);
    constexpr int S = (TPG / NW) * SPCT / 100;
    static_assert(S >= NL + 8 && (TPG & (TPG - 1)) == 0, "queue");
    const int r = lane >> 3, c = lane & 7, rnk = j * 4 + wv;
    const unsigned loff = r * 384 + c * 16;
    const rsrc_t rk = mk((const char*)Kc + (size_t)h * SK * 384, SK * 384), rv = mk((const char*)Vc + (size_t)h * SK * 384, SK * 384);
    const unsigned* cp = (const unsigned*)(ws + WS_CTR + h * 256);
    u32x4 A[2 * U][3], B[2 * U][3], C[2 * U][3];
#define TOFF(tile) ((unsigned)(((tile) + h * ROT) & (TPG - 1)) * (3072u * U))
#define LOADT(X, tile) do { const unsigned so_ = TOFF(tile), vo_ = (tile) < TPG ? loff : OOB; \
    _Pragma("unroll") for (int u = 0; u < 2 * U; ++u) _Pragma("unroll") for (int q_ = 0; q_ < 3; ++q_) \
      X[u][q_] = __builtin_amdgcn_raw_buffer_load_b128(u < U ? rk : rv, vo_ + (u % U) * 3072 + q_ * 128, so_, AUX_NT); } while (0)
#define LOADP(X, tile, i0) do { const unsigned so_ = TOFF(tile); \
    _Pragma("unroll") for (int u = 0; u < 2 * U; ++u) _Pragma("unroll") for (int q_ = 0; q_ < 3; ++q_) { \
      if ((i0) + u * 3 + q_ >= PF) asm volatile("s_waitcnt vmcnt(%0)" :: "i"(PF > 63 ? 63 : PF - 1) : "memory"); \
      X[u][q_] = __builtin_amdgcn_raw_buffer_load_b128(u < U ? rk : rv, loff + (u % U) * 3072 + q_ * 128, so_, AUX_NT); } } while (0)
#define USE(X) do { float s_[U]; float mn_ = m; \
    _Pragma("unroll") for (int u = 0; u < U; ++u) { float d_ = 0.f; \
      _Pragma("unroll") for (int q_ = 0; q_ < 3; ++q_) _Pragma("unroll") for (int i = 0; i < 4; ++i) d_ = dot2(X[u][q_][i], qv[q_][i], d_); \
      d_ += dpp<XOR1>(d_); d_ += dpp<XOR2>(d_); d_ += dpp<HMIR>(d_); \
      s_[u] = rbf(rbf(d_) * 0.07216878364870322f); mn_ = fmaxf(mn_, s_[u]); } \
    const float al_ = __expf(m - mn_); m = mn_; l *= al_; \
    _Pragma("unroll") for (int i = 0; i < 24; ++i) acc[i] *= al_; \
    _Pragma("unroll") for (int u = 0; u < U; ++u) { const float p_ = __expf(s_[u] - mn_); l += p_; \
      _Pragma("unroll") for (int q_ = 0; q_ < 3; ++q_) _Pragma("unroll") for (int i = 0; i < 4; ++i) { \
        acc[q_ * 8 + 2 * i] = fmaf(p_, lo16(X[U + u][q_][i]), acc[q_ * 8 + 2 * i]); acc[q_ * 8 + 2 * i + 1] = fmaf(p_, hi16(X[U + u][q_][i]), acc[q_ * 8 + 2 * i + 1]); } } } while (0)
#define GRAB(gg) do { u64 sv_; asm volatile("s_mov_b64 %1, exec\n\ts_mov_b64 exec, 1\n\tglobal_atomic_add %0, %2, %3, %4 sc0\n\ts_mov_b64 exec, %1" : "=&v"(gg), "=&s"(sv_) : "v"(0u), "v"(1u), "s"(cp) : "memory"); } while (0)
    LOADP(A, rnk, 0);
    if (j < 2) bar();
    LOADP(B, NW + rnk, 6 * U);
    LOADP(C, 2 * NW + rnk, 12 * U);
    const unsigned lbase = (unsigned)(L_KV + wv * KVW) * 4u;
#pragma unroll
    for (int t = 0; t < NL; ++t) {
      const unsigned so_ = TOFF((3 + t) * NW + rnk);
#pragma unroll
      for (int u = 0; u < 2 * U; ++u)
#pragma unroll
        for (int q_ = 0; q_ < 3; ++q_) {
          unsigned keep;
          asm volatile("s_waitcnt vmcnt(%6)\n\ts_mov_b32 %0, m0\n\ts_mov_b32 m0, %1\n\ts_nop 4\n\tbuffer_load_dwordx4 %2, %3, %4 offen nt lds\n\ts_mov_b32 m0, %0"
                       : "=&s"(keep) : "s"(lbase + (unsigned)((t * 2 * U + u) * 3 + q_) * 1024u), "v"(loff), "s"(u < U ? rk : rv),
                         "s"(so_ + (unsigned)((u % U) * 3072 + q_ * 128)), "i"(0), "i"(PF > 63 ? 63 : PF - 1) : "memory");
        }
    }
    if (wv == 0) STAMP(8);
    if (j >= 2) bar();
    u32x4 qv[3];
#pragma unroll
    for (int q_ = 0; q_ < 3; ++q_) qv[q_] = *reinterpret_cast<const u32x4*>(lds + L_Q + q_ * 32 + c * 4);
    if (wv == 0) STAMP(1);
    float m = -INFINITY, l = 0.f, acc[24];
#pragma unroll
    for (int i = 0; i < 24; ++i) acc[i] = 0.f;
    int tA = 0, tB = 0, tC = 0, n = 3 + NL;
    unsigned gA = 0, gB = 0, gC = 0;
#define NEXT(X, tX, gX, tP, gN) do { \
      asm volatile("s_waitcnt vmcnt(%1)" : "+v"(gX) : "i"(12 * U) : "memory"); \
      const int lim_ = (n & 1) ? TPG - ZEND : TPG;     \
      tX = tP >= lim_ ? TPG : n >= S ? S * NW + (int)__builtin_amdgcn_readfirstlane(gX) : n * NW + rnk; ++n; \
      if (n + 1 >= S && tX < lim_) GRAB(gN); \
      LOADT(X, tX); \
      asm volatile("" ::: "memory"); } while (0)
    do {
      if (tA < TPG) USE(A);
      NEXT(A, tA, gA, tB, gC);
      if (tB < TPG) USE(B);
      NEXT(B, tB, gB, tC, gA);
      if (tC < TPG) USE(C);
      if (NL > 0 && n == 5 + NL) {
        asm volatile("s_waitcnt vmcnt(%0)" :: "i"(12 * U) : "memory");
#pragma unroll 1
        for (int t = 0; t < NL; ++t) {
#pragma unroll
          for (int u = 0; u < 2 * U; ++u)
#pragma unroll
            for (int q_ = 0; q_ < 3; ++q_) C[u][q_] = *reinterpret_cast<const u32x4*>(lds + L_KV + wv * KVW + ((t * 2 * U + u) * 3 + q_) * 256 + lane * 4);
          USE(C);
        }
      }
      NEXT(C, tC, gC, tA, gB);
    } while (tA < TPG || tB < TPG || tC < TPG);
    const float M = max_hi3(m);
    const float sc = __expf(m - M);
    const float L = sum_hi3(l * sc);
    float y1[12], y2[6], y3[3];
#pragma unroll
    for (int k = 0; k < 12; ++k) y1[k] = tr32(acc[k] * sc, acc[k + 12] * sc);
#pragma unroll
    for (int k = 0; k < 6; ++k) y2[k] = tr16(y1[k], y1[k + 6]);
#pragma unroll
    for (int k = 0; k < 3; ++k) y3[k] = tr8(y2[k], y2[k + 3], (lane & 8) != 0);
    const int ai0 = 12 * (lane >> 5) + 6 * ((lane >> 4) & 1) + 3 * ((lane >> 3) & 1);
#pragma unroll
    for (int k = 0; k < 3; ++k) { const int ai = ai0 + k; ldf[L_PART + wv * 200 + (ai >> 3) * 64 + c * 8 + (ai & 7)] = y3[k]; }
    if (lane == 0) { ldf[L_PART + wv * 200 + 192] = M; ldf[L_PART + wv * 200 + 193] = L; }
    if (wv == 0) STAMP(2);
    bar();
    return;
  }

  const int vt = tid - 256, vw = wv - 4;
  if (PRIO) __builtin_amdgcn_s_setprio(PRIO);
  float* wred = ldf + L_RED + vw * 480;
  const u64 E1 = (((u64)ev[1] << 32) | ev[0]) + 1ull;
  Tag tg; tg.tl = (unsigned)(E1 & 0xffffu) << 16; tg.th = (unsigned)(E1 >> 16);
  {
    const int ql = lane < 48 ? lane : 47;
    u32x4 g[2];
    gather<2>(rws, WS_P2 + (h * 2 * 192 + 48 * vw + ql) * 16, 192 * 16, tg, g);
    if (lane < 48) reinterpret_cast<u16*>(lds + L_Q)[48 * vw + lane] = (u16)bfbits(dec2(g[0]) + dec2(g[1]));
  }
  if (vw == 0) STAMP(14);
  bar();

  if (j == 4 || j == 5) {
    const int m2 = 2 * h + j - 4;
    const unsigned ob = lane < 50 ? 0u : OOB;
    u32x4 w[16]; unsigned cp2[8];
#pragma unroll
    for (int k = 0; k < 8; ++k) cp2[k] = __builtin_amdgcn_raw_buffer_load_b32(rc, (160 * vw + 2 * (mrg + 10 * k)) * 2 | ob, 0, 0);
#pragma unroll
    for (int k = 0; k < 16; ++k) w[k] = __builtin_amdgcn_raw_buffer_load_b128(rmod, ((160 * vw + 2 * (mrg + 10 * (k >> 1)) + (k & 1)) * (6 * D) + 2 * D + 40 * m2 + 8 * mcc) * 2 | ob, 0, AUX_NT);
    float a8[8];
#pragma unroll
    for (int i = 0; i < 8; ++i) a8[i] = 0.f;
#pragma unroll
    for (int k = 0; k < 8; ++k) {
      const unsigned sp = bfbits(silu_f(lo16(cp2[k]))) | (bfbits(silu_f(hi16(cp2[k]))) << 16);
#pragma unroll
      for (int i = 0; i < 4; ++i) { a8[2 * i] = dot2(plo(w[2 * k + 1][i], w[2 * k][i]), sp, a8[2 * i]); a8[2 * i + 1] = dot2(phi(w[2 * k + 1][i], w[2 * k][i]), sp, a8[2 * i + 1]); }
    }
    if (lane < 50) {
      *reinterpret_cast<float4*>(wred + mrg * 40 + mcc * 8) = float4{a8[0], a8[1], a8[2], a8[3]};
      *reinterpret_cast<float4*>(wred + mrg * 40 + mcc * 8 + 4) = float4{a8[4], a8[5], a8[6], a8[7]};
    }
    lds_wait();
    unsigned old = 0;
    if (lane == 0) old = __hip_atomic_fetch_add(lds + L_CNT + 1, 1u, __ATOMIC_RELAXED, __HIP_MEMORY_SCOPE_WORKGROUP);
    old = __builtin_amdgcn_readfirstlane(old);
    asm volatile("" ::: "memory");
    if (old == 3u) {
      float p4[4] = {0.f, 0.f, 0.f, 0.f};
#pragma unroll
      for (int g = 0; g < 10; ++g)
#pragma unroll
        for (int w4 = 0; w4 < 4; ++w4) p4[w4] += ldf[L_RED + w4 * 480 + g * 40 + gl];
      if (lane < 40) st8(rws, WS_M2 + (40 * m2 + lane) * 8, u32x2{bfbits((p4[0] + p4[1]) + (p4[2] + p4[3])) | tg.tl, tg.th});
      lds_wait();
      if (lane == 0) __hip_atomic_store(lds + L_CNT + 1, 8u, __ATOMIC_RELAXED, __HIP_MEMORY_SCOPE_WORKGROUP);
    }
    for (unsigned spin = 0; spin < (SPIN_LIMIT << 4); ++spin) {
      if (__hip_atomic_load(lds + L_CNT + 1, __ATOMIC_RELAXED, __HIP_MEMORY_SCOPE_WORKGROUP) >= 8u) break;
      asm volatile("s_sleep 2" ::: "memory");
    }
    asm volatile("" ::: "memory");
  }
  const int org = lane / 10, ocg = lane - 10 * org;
  unsigned wop[4][8];
  {
    const rsrc_t rwo = mk(wo, HD * D * 2);
    const unsigned o0 = lane < 60 ? ((h * DH + 48 * vw + 8 * org) * D + 80 * j + 8 * ocg) * 2 : OOB;
    u32x4 w[8];
#pragma unroll
    for (int k = 0; k < 8; ++k) w[k] = __builtin_amdgcn_raw_buffer_load_b128(rwo, o0, k * D * 2, AUX_NT);
#pragma unroll
    for (int p = 0; p < 4; ++p)
#pragma unroll
      for (int i = 0; i < 4; ++i) { wop[p][2 * i] = plo(w[2 * p + 1][i], w[2 * p][i]); wop[p][2 * i + 1] = phi(w[2 * p + 1][i], w[2 * p][i]); }
  }
  auto h1_stage = [&]() {
    if (vw == 0) STAMP(9);
    bar();
    if (vw == 0) STAMP(4);
    {
      const float m0 = ldf[L_PART + 192], m1 = ldf[L_PART + 200 + 192], m2 = ldf[L_PART + 400 + 192], m3 = ldf[L_PART + 600 + 192];
      const float M = fmaxf(fmaxf(m0, m1), fmaxf(m2, m3));
      const float e0 = __expf(m0 - M), e1 = __expf(m1 - M), e2 = __expf(m2 - M), e3 = __expf(m3 - M);
      const int idx = vt < 192 ? vt : 193;
      float v = e0 * ldf[L_PART + idx];
      v = fmaf(e1, ldf[L_PART + 200 + idx], v); v = fmaf(e2, ldf[L_PART + 400 + idx], v); v = fmaf(e3, ldf[L_PART + 600 + idx], v);
      if (vt == 192) v = M;
      if (vt < 194) st16(rws, WS_R + (b * 200 + vt) * 16, enc2(v, tg));
    }
    {
      const int s = lane >> 3, dg = lane & 7;
      u32x4 g[8];
      const unsigned rb = WS_R + (h * 8 + s) * 200 * 16, r0 = rb + (48 * vw + dg) * 16;
      poll<8>([&](int k) { return __builtin_amdgcn_raw_buffer_load_b128(rws, k < 6 ? r0 + k * 128 : rb + (186 + k) * 16, 0, AUX_SC1); }, tg, g);
      if (vw == 0) STAMP(15);
      const float ms = dec2(g[6]), M = max_hi3(ms), e = __expf(ms - M);
      const float L = sum_hi3(e * dec2(g[7])), rl = __builtin_amdgcn_rcpf(L);
      float z[3];
#pragma unroll
      for (int k = 0; k < 3; ++k) z[k] = tr32(e * dec2(g[k]), e * dec2(g[k + 3]));
      const float z01 = tr16(z[0], z[1]), z2 = tr16(z[2], z[2]);
      const float ov = tr8(z01, z2, (lane & 8) != 0);
      u16* op = reinterpret_cast<u16*>(lds + L_OP + vw * 24) + dg + 24 * (lane >> 5);
      if ((lane & 24) != 24) op[(lane & 8) ? 16 : 8 * ((lane >> 4) & 1)] = (u16)bfbits(fdiv(ov, L, rl));
      lds_wait();
      const u32x4 opv = *reinterpret_cast<const u32x4*>(lds + L_OP + vw * 24 + 4 * (org < 6 ? org : 0));
      float y[8];
#pragma unroll
      for (int i = 0; i < 8; ++i) y[i] = 0.f;
#pragma unroll
      for (int p = 0; p < 4; ++p)
#pragma unroll
        for (int i = 0; i < 8; ++i) y[i] = dot2(wop[p][i], opv[p], y[i]);
      if (lane < 60) {
        float* dst = ldf + L_RED + (vw * 6 + org) * 80 + 8 * ocg;
        *reinterpret_cast<float4*>(dst) = float4{y[0], y[1], y[2], y[3]};
        *reinterpret_cast<float4*>(dst + 4) = float4{y[4], y[5], y[6], y[7]};
      }
    }
    bar();
    if (vw < 3) {
      const int yc = vt < 160 ? vt >> 1 : 79;
      const float* src = ldf + L_RED + (vt & 1) * 960 + yc;
      float p4[4] = {0.f, 0.f, 0.f, 0.f};
#pragma unroll
      for (int g = 0; g < 12; ++g) p4[g & 3] += src[g * 80];
      float y = (p4[0] + p4[1]) + (p4[2] + p4[3]);
      y += dpp<XOR1>(y);
      if (vt < 160 && (vt & 1) == 0) st16(rws, WS_Y + (((20 * j + (yc >> 2)) * 32 + h) * 4 + (yc & 3)) * 16, enc2(y, tg));
    }
    if (vw == 0) STAMP(5);
  };

  if (j == 2 || j == 3) {
    const int f = 2 * h + j - 2, rg = vt >> 3, cc = vt & 7;
    if (vw < 3) {
      const rsrc_t rwd = mk(wd, F * D * 2);
      const unsigned o0 = vt < 160 ? (24 * f * D + 4 * vt) * 2 : OOB;
      u32x2 w[24];
#pragma unroll
      for (int k = 0; k < 24; ++k) w[k] = __builtin_amdgcn_raw_buffer_load_b64(rwd, o0, k * D * 2, AUX_NT);
      if (vt < 160) {
#pragma unroll
        for (int p = 0; p < 12; ++p)
          *reinterpret_cast<u32x4*>(lds + L_WD + (p * 160 + vt) * 4) =
              u32x4{plo(w[2 * p + 1][0], w[2 * p][0]), phi(w[2 * p + 1][0], w[2 * p][0]), plo(w[2 * p + 1][1], w[2 * p][1]), phi(w[2 * p + 1][1], w[2 * p][1])};
      }
    }
    unsigned gp[10][8];
    {
      const rsrc_t rg_ = mk(wg, D * F * 2), ru_ = mk(wu, D * F * 2);
      const unsigned og = cc < 3 ? (20 * rg * F + 24 * f + 8 * cc) * 2 : OOB, ou = (cc >= 3 && cc < 6) ? (20 * rg * F + 24 * f + 8 * (cc - 3)) * 2 : OOB;
#pragma unroll
      for (int hf = 0; hf < 2; ++hf) {
        u32x4 w[10], w2[10];
#pragma unroll
        for (int k = 0; k < 10; ++k) {
          w[k] = __builtin_amdgcn_raw_buffer_load_b128(rg_, og, (10 * hf + k) * F * 2, AUX_NT);
          w2[k] = __builtin_amdgcn_raw_buffer_load_b128(ru_, ou, (10 * hf + k) * F * 2, AUX_NT);
        }
#pragma unroll
        for (int k = 0; k < 10; ++k) w[k] |= w2[k];
#pragma unroll
        for (int p = 0; p < 5; ++p)
#pragma unroll
          for (int i = 0; i < 4; ++i) { gp[5 * hf + p][2 * i] = plo(w[2 * p + 1][i], w[2 * p][i]); gp[5 * hf + p][2 * i + 1] = phi(w[2 * p + 1][i], w[2 * p][i]); }
      }
#pragma unroll
      for (int p = 0; p < 10; ++p)
#pragma unroll
        for (int i = 0; i < 8; ++i) asm volatile("" : "+v"(gp[p][i]));
    }
    unsigned po[5];
#pragma unroll
    for (int k = 0; k < 5; ++k) { const int p = 80 * vw + lane + 64 * k; po[k] = (unsigned)(p >= 320 ? p - 320 : p) * 16u; }
    float sbv[4], c1v[4];
    {
      u32x4 g[4];
      const unsigned off[4] = {WS_M2 + D * 8 + po[0], WS_M2 + D * 8 + po[1], WS_M2 + 2 * D * 8 + po[0], WS_M2 + 2 * D * 8 + po[1]};
      gather_slow<4>(rws, off, tg, g);
#pragma unroll
      for (int k = 0; k < 2; ++k) {
        const unsigned sp = decp(g[k]), cq = decp(g[2 + k]);
        sbv[2 * k] = lo16(sp); sbv[2 * k + 1] = hi16(sp); c1v[2 * k] = rbf(1.f + lo16(cq)); c1v[2 * k + 1] = rbf(1.f + hi16(cq));
      }
    }
    h1_stage();
    u32x4 wdr[12];
#pragma unroll
    for (int p = 0; p < 12; ++p) wdr[p] = *reinterpret_cast<const u32x4*>(lds + L_WD + (p * 160 + (vt < 160 ? vt : 0)) * 4);
    {
      u32x4 g[5];
      poll<5>([&](int k) { return __builtin_amdgcn_raw_buffer_load_b128(rws, WS_X1 + po[k], 0, AUX_SC1); }, tg, g);
      if (vw == 0) STAMP(6);
      float xv[10];
#pragma unroll
      for (int k = 0; k < 5; ++k) { xv[2 * k] = __uint_as_float(g[k][0] << 16); xv[2 * k + 1] = __uint_as_float(g[k][2] << 16); }
      const float s = ((xv[0] + xv[1]) + (xv[2] + xv[3])) + ((xv[4] + xv[5]) + (xv[6] + xv[7])) + (xv[8] + xv[9]);
      const float mean = sum_wave(s) * (1.f / D);
      float v0 = 0.f, v1 = 0.f;
#pragma unroll
      for (int k = 0; k < 5; ++k) { const float d0 = xv[2 * k] - mean, d1 = xv[2 * k + 1] - mean; v0 = fmaf(d0, d0, v0); v1 = fmaf(d1, d1, v1); }
      const float var = sum_wave(v0 + v1) * (1.f / D);
      const float mb2 = rbf(mean), den2 = rbf(sqrtf(rbf(rbf(var) + 1e-5f))), rd = __builtin_amdgcn_rcpf(den2);
#pragma unroll
      for (int k = 0; k < 2; ++k) {
        const float n0 = rbf(fdiv(rbf(xv[2 * k] - mb2), den2, rd)), n1 = rbf(fdiv(rbf(xv[2 * k + 1] - mb2), den2, rd));
        const float h0 = rbf(n0 * c1v[2 * k]) + sbv[2 * k], h1 = rbf(n1 * c1v[2 * k + 1]) + sbv[2 * k + 1];
        if (k == 0 || lane < 16) lds[L_H2 + vw * 80 + lane + 64 * k] = bfbits(h0) | (bfbits(h1) << 16);
      }
    }
    lds_wait();
    {
      float a8[8];
#pragma unroll
      for (int i = 0; i < 8; ++i) a8[i] = 0.f;
#pragma unroll
      for (int p2 = 0; p2 < 5; ++p2) {
        const u32x2 hp = *reinterpret_cast<const u32x2*>(lds + L_H2 + vw * 80 + 10 * (lane >> 3) + 2 * p2);
#pragma unroll
        for (int i = 0; i < 8; ++i) { a8[i] = dot2(gp[2 * p2][i], hp[0], a8[i]); a8[i] = dot2(gp[2 * p2 + 1][i], hp[1], a8[i]); }
      }
      float z1[4], z2[2];
#pragma unroll
      for (int k = 0; k < 4; ++k) z1[k] = tr32(a8[k], a8[k + 4]);
#pragma unroll
      for (int k = 0; k < 2; ++k) z2[k] = tr16(z1[k], z1[k + 2]);
      const float z3 = tr8(z2[0], z2[1], (lane & 8) != 0);
      ldf[L_GU + vw * 64 + cc * 8 + 4 * (lane >> 5) + 2 * ((lane >> 4) & 1) + ((lane >> 3) & 1)] = z3;
    }
    bar();
    if (vw < 3) {
      const int n = lane < 24 ? lane : 0;
      const float g = (ldf[L_GU + n] + ldf[L_GU + 64 + n]) + (ldf[L_GU + 128 + n] + ldf[L_GU + 192 + n]);
      const float u = (ldf[L_GU + 24 + n] + ldf[L_GU + 88 + n]) + (ldf[L_GU + 152 + n] + ldf[L_GU + 216 + n]);
      const unsigned ab = bfbits(rbf(silu_f(rbf(g))) * rbf(u));
      const unsigned nb = (unsigned)__builtin_amdgcn_mov_dpp((int)ab, XOR1, 0xf, 0xf, true);
      const unsigned ap = ab | (nb << 16);
      float y[4] = {0.f, 0.f, 0.f, 0.f};
#pragma unroll
      for (int p = 0; p < 12; ++p) {
        const unsigned a2 = (unsigned)__builtin_amdgcn_readlane((int)ap, 2 * p);
#pragma unroll
        for (int i = 0; i < 4; ++i) y[i] = dot2(wdr[p][i], a2, y[i]);
      }
      const bool o1 = (lane & 1) != 0, o2 = (lane & 2) != 0;
      { const float s0 = o1 ? y[0] : y[1], s1 = o1 ? y[2] : y[3]; const float r0 = dpp<XOR1>(s0), r1 = dpp<XOR1>(s1);
        if (o1) { y[0] = r0; y[2] = r1; } else { y[1] = r0; y[3] = r1; } }
      { const float s0 = o2 ? y[0] : y[2], s1 = o2 ? y[1] : y[3]; const float r0 = dpp<XOR2>(s0), r1 = dpp<XOR2>(s1);
        if (o2) { y[0] = r0; y[1] = r1; } else { y[2] = r0; y[3] = r1; } }
      if (vt < 160) {
#pragma unroll
        for (int i = 0; i < 4; ++i) st16(rws, WS_O + ((((vt & ~3) + i) * NFFN + f) * 4 + (vt & 3)) * 16, enc2(y[i], tg));
      }
    }
    if (vw == 0) STAMP(10);
  } else {
    const bool red = j != 4 && vw == 3;
    const int rr = 5 * h + (j < 2 ? j : j - 3), col = 4 * rr + (lane & 3);
    float ga = 0.f, gb = 0.f, xv = 0.f;
    if (red) {
      u32x4 g[2];
      const unsigned off[2] = {WS_M2 + (col & ~1) * 8, WS_M2 + (col & ~1) * 8 + 3 * D * 8};
      gather_slow<2>(rws, off, tg, g);
      const unsigned pa = decp(g[0]), pb = decp(g[1]);
      ga = (col & 1) ? hi16(pa) : lo16(pa); gb = (col & 1) ? hi16(pb) : lo16(pb);
      xv = __uint_as_float((unsigned)x[col] << 16);
    }
    h1_stage();
    if (red) {
      float x1;
      {
        u32x4 g[2];
        gather<2>(rws, WS_Y + (rr * 128 + lane) * 16, 64 * 16, tg, g);
        float y = dec2(g[0]) + dec2(g[1]);
        y += dpp<ROR4>(y); y = sum_hi3(y);
        x1 = rbf(xv + rbf(ga * rbf(y)));
        if (lane < 4) st8(rws, WS_X1 + col * 8, u32x2{bfbits(x1) | tg.tl, tg.th});
      }
      STAMP(3);
      {
        u32x4 g[4];
        gather<4>(rws, WS_O + (rr * NFFN * 4 + lane) * 16, 64 * 16, tg, g);
        float f = (dec2(g[0]) + dec2(g[1])) + (dec2(g[2]) + dec2(g[3]));
        f += dpp<ROR4>(f); f = sum_hi3(f);
        const float o = x1 + rbf(gb * rbf(f));
        if (lane < 4) out[col] = (u16)bfbits(o);
        if (b == 0 && lane == 0) *reinterpret_cast<u64*>(ws + WS_EPOCH) = E1;
      }
      STAMP(7);
    }
  }
}

extern "C" void kernel_launch(void* const* d_in, const int* in_sizes, int n_in, void* d_out, int out_size, void* d_ws, size_t ws_size, hipStream_t stream) {
  (void)in_sizes; (void)n_in; (void)out_size; (void)ws_size;
  k_block<<<NWG, 512, 0, stream>>>((const u16*)d_in[0], (const u16*)d_in[1], (const u16*)d_in[2], (const u16*)d_in[3], (const u16*)d_in[4],
                                   (const u16*)d_in[6], (const u16*)d_in[7], (const u16*)d_in[8], (const u16*)d_in[9], (const u16*)d_in[10],
                                   (u16*)d_out, (unsigned char*)d_ws);
}
```
